```python
import torch

def _softmax(x, axis=-1):
    return torch.softmax(x, dim=axis)
def _gelu(x):
    return torch.nn.functional.gelu(x, approximate="tanh")
def _silu(x):
    return torch.nn.functional.silu(x)
def _sigmoid(x):
    return torch.sigmoid(x)
def _layernorm(x, axis=-1, eps=1e-5):
    m = x.mean(dim=axis, keepdim=True)
    v = x.var(dim=axis, keepdim=True, unbiased=False)
    return (x - m) / torch.sqrt(v + eps)
def _scatter_add(tgt, idx, src, axis=0):
    return tgt.clone().index_add_(axis, idx, src)
def _segment_sum(src, seg, n):
    out = torch.zeros((n,) + src.shape[1:], dtype=src.dtype)
    return out.index_add_(0, seg, src)
def _pad(x, pw):
    flat = tuple(v for pair in reversed(pw) for v in pair)
    return torch.nn.functional.pad(x, flat)

kernel_name = 'sequential_preLN__softmax_full_mha__dense_activation_silu__prompt'

def setup_inputs():

    _w = (lambda a: (lambda t: t.to(torch.bfloat16) if t.is_floating_point() else t)(torch.as_tensor(a)))
    def _A_setup():
        B, S, D = 1, 2048, 448
        return {
            "x": _w((lambda *s: torch.randn(*s, dtype=torch.bfloat16) * 0.05)(B, S, D)),
            "wqkv": _w((lambda *s: torch.randn(*s, dtype=torch.bfloat16) * 0.05)(D, 3 * D)),
            "wo": _w((lambda *s: torch.randn(*s, dtype=torch.bfloat16) * 0.05)(D, D)),
        }
    _A_in = _A_setup()

    def _B_setup():
        B, S, D, F = 1, 2048, 448, 1792
        return {
            "x": _w((lambda *s: torch.randn(*s, dtype=torch.bfloat16) * 0.05)(B, S, D)),
            "w1": _w((lambda *s: torch.randn(*s, dtype=torch.bfloat16) * 0.8)(D, F) * (D ** -0.5)),
            "w2": _w((lambda *s: torch.randn(*s, dtype=torch.bfloat16) * 0.2)(F, D) * (F ** -0.5)),
        }
    _B_in = _B_setup()

    return {
        "A_x": _A_in["x"],
        "A_wqkv": _A_in["wqkv"],
        "A_wo": _A_in["wo"],
        "B_x": _B_in["x"],
        "B_w1": _B_in["w1"],
        "B_w2": _B_in["w2"],
    }

def reference(A_x, A_wqkv, A_wo, B_x, B_w1, B_w2):
    def _A_ref(x, wqkv, wo):
        B, S, D = x.shape
        H = 16; Dh = D // H
        qkv = (lambda x, *d: x.permute(*d))((x @ wqkv).reshape(B, S, 3, H, Dh), 2, 0, 3, 1, 4)
        q, k, v = qkv[0], qkv[1], qkv[2]
        scale = Dh ** -0.5
        scores = torch.einsum("bhqd,bhkd->bhqk", q, k) * scale
        p = _softmax(scores, axis=-1)
        o = torch.einsum("bhqk,bhkd->bhqd", p, v)
        return (lambda x, *d: x.permute(*d))(o, 0, 2, 1, 3).reshape(B, S, D) @ wo

    def _B_ref(x, w1, w2):
        h = torch.einsum("bsd,df->bsf", x, w1)
        h = _silu(h)
        out = torch.einsum("bsf,fd->bsd", h, w2)
        return out

    x = A_x
    h = x + _A_ref(_layernorm(x, axis=-1), A_wqkv, A_wo)
    h2 = h + _B_ref(_layernorm(h, axis=-1), B_w1, B_w2)
    return h2
```

```cpp
#include <hip/hip_runtime.h>
#include <hip/hip_bf16.h>
#include <stdint.h>

typedef short bf16x8 __attribute__((ext_vector_type(8)));
typedef short s16x4 __attribute__((ext_vector_type(4)));
typedef float f32x16 __attribute__((ext_vector_type(16)));
typedef float f32x4 __attribute__((ext_vector_type(4)));
typedef float f32x2 __attribute__((ext_vector_type(2)));
typedef unsigned u32x4 __attribute__((ext_vector_type(4)));
typedef unsigned u32x2 __attribute__((ext_vector_type(2)));
typedef __bf16 bf16x2_t __attribute__((ext_vector_type(2)));
#define LAS __attribute__((address_space(3)))
#define DI __device__ __forceinline__
#define MFMA(a, b, c) __builtin_amdgcn_mfma_f32_32x32x16_bf16(__builtin_bit_cast(bf16x8, (a)), __builtin_bit_cast(bf16x8, (b)), (c), 0, 0, 0)
#define BAR() asm volatile("s_waitcnt lgkmcnt(0)\n\ts_barrier" ::: "memory")

constexpr int SEQ = 2048, DM = 448, NH = 16, DH = 28, FF = 1792;
constexpr float EPS = 1e-5f;
constexpr float QSCALE = 0.18898223650461362f * 1.4426950408889634f;
constexpr size_t KB = 1024;
constexpr size_t QF_OFF = 0;
constexpr size_t KV_OFF = QF_OFF + 16 * 64 * 2 * KB;
constexpr size_t OF_OFF = KV_OFF + 16 * 64 * 4 * KB;
constexpr size_t WO_OFF = OF_OFF + 64 * 32 * KB;
constexpr size_t HF_OFF = WO_OFF + 14 * 32 * KB;
constexpr size_t W1_OFF = HF_OFF + 64 * 28 * KB;
constexpr size_t UF_OFF = W1_OFF + 56 * 28 * KB;
constexpr size_t W2_OFF = UF_OFF + 64 * 112 * KB;
constexpr size_t CS_OFF = W2_OFF + 14 * 112 * KB;

DI unsigned pk2(float lo, float hi) { f32x2 v = {lo, hi}; return __builtin_bit_cast(unsigned, __builtin_convertvector(v, bf16x2_t)); }
DI float dot2(unsigned a, unsigned b, float c) { return __builtin_amdgcn_fdot2_f32_bf16(__builtin_bit_cast(bf16x2_t, a), __builtin_bit_cast(bf16x2_t, b), c, false); }
DI float bflo(unsigned u) { return __uint_as_float(u << 16); }
DI float bfhi(unsigned u) { return __uint_as_float(u & 0xffff0000u); }
DI float swap_add(float v) { auto r = __builtin_amdgcn_permlane32_swap(__float_as_uint(v), __float_as_uint(v), false, false); return __uint_as_float(r[0]) + __uint_as_float(r[1]); }
DI float swap_max(float v) { auto r = __builtin_amdgcn_permlane32_swap(__float_as_uint(v), __float_as_uint(v), false, false); return fmaxf(__uint_as_float(r[0]), __uint_as_float(r[1])); }
DI void glds16(const void* g, unsigned lds_base) {
    unsigned sv;
    asm volatile("s_mov_b32 %0, m0\n\ts_mov_b32 m0, %2\n\ts_nop 0\n\tglobal_load_lds_dwordx4 %1, off\n\ts_mov_b32 m0, %0" : "=&s"(sv) : "v"(g), "s"(lds_base) : "memory");
}
typedef short v4i16_t __attribute__((ext_vector_type(4)));
DI u32x2 trd(LAS const char* p) { return __builtin_bit_cast(u32x2, __builtin_amdgcn_ds_read_tr16_b64_v4i16((LAS v4i16_t*)p)); }
DI u32x4 trfrag(LAS const char* p) { u32x2 a = trd(p), b = trd(p + 256); return u32x4{a.x, a.y, b.x, b.y}; }
DI u32x4 pack8(const f32x16& a, int s) {
    return u32x4{pk2(a[8 * s], a[8 * s + 1]), pk2(a[8 * s + 2], a[8 * s + 3]), pk2(a[8 * s + 4], a[8 * s + 5]), pk2(a[8 * s + 6], a[8 * s + 7])};
}
DI float max16(const f32x16& p) {
    float a = fmaxf(fmaxf(p[0], p[1]), p[2]), b = fmaxf(fmaxf(p[3], p[4]), p[5]);
    a = fmaxf(fmaxf(a, p[6]), p[7]); b = fmaxf(fmaxf(b, p[8]), p[9]);
    a = fmaxf(fmaxf(a, p[10]), p[11]); b = fmaxf(fmaxf(b, p[12]), p[13]);
    a = fmaxf(fmaxf(a, p[14]), p[15]);
    return fmaxf(a, b);
}
DI u32x2 split3(float m) {
    const float v = -m;
    const unsigned u1 = __float_as_uint(v) & 0xffff0000u; const float r1 = v - __uint_as_float(u1);
    const unsigned u2 = __float_as_uint(r1) & 0xffff0000u; const float r2 = r1 - __uint_as_float(u2);
    const unsigned u3 = __float_as_uint(r2) & 0xffff0000u;
    return u32x2{(u1 >> 16) | u2, u3 >> 16};
}

constexpr int K1_W = 0, K1_CS = 84 * 1024, K1_ST = K1_CS + 4 * 3 * 64 * 4, K1_C1 = K1_ST + 4 * 64 * 4, K1_LDS = K1_C1 + 4 * 64 * 4;
__global__ __launch_bounds__(512, 2) void k1_qkv(const unsigned short* __restrict__ x, const unsigned short* __restrict__ wqkv, const unsigned short* __restrict__ wo,
                                                 const unsigned short* __restrict__ w1, const unsigned short* __restrict__ w2, char* __restrict__ ws) {
    extern __shared__ __attribute__((aligned(16))) char smem[];
    LAS char* lds = (LAS char*)smem;
    const int tid = threadIdx.x, lane = tid & 63, w = __builtin_amdgcn_readfirstlane(tid >> 6), r = lane & 31, hi = lane >> 5;
    const int b = blockIdx.x, xcd = b & 7, bi = b >> 3;
    const int rb = (xcd >> 1) * 4 + (bi & 3), h = (xcd & 1) * 8 + (bi >> 2);
    LAS const char* trp = lds + K1_W + (8 * hi + ((lane & 15) >> 2)) * 64 + ((lane >> 4) & 1) * 32 + (lane & 3) * 8;
    if (w >= 4) {
        const int L = w - 4;
        const unsigned lds0 = (unsigned)(uintptr_t)smem;
        const int pc = lane & 3;
        const char* src0 = (const char*)wqkv + (size_t)(16 * L + (lane >> 2)) * (3 * DM * 2) + h * (DH * 2) + (pc == 3 ? 40 : pc * 16);
        const unsigned short* src; int ldn; char* dst; int kind;
        int f0;
        if (b < 112) { src = w1 + (b >> 1) * 32; ldn = FF; f0 = (b & 1) * 14; dst = ws + W1_OFF + (size_t)((b >> 1) * 28 + f0) * KB; kind = 0; }
        else if (b < 224) { const int q = b - 112; src = w2 + (q >> 3) * 32; ldn = DM; f0 = (q & 7) * 14; dst = ws + W2_OFF + (size_t)((q >> 3) * 112 + f0) * KB; kind = 0; }
        else { const int q = (b - 224) * 14; src = wo; ldn = DM; f0 = q; dst = ws + WO_OFF + (size_t)q * KB; kind = 1; }
        unsigned short e[4][8];
#pragma unroll
        for (int i = 0; i < 4; ++i) {
            const int fi = (L + 4 * i < 14) ? L + 4 * i : L;
            const int f = f0 + fi;
#pragma unroll
            for (int j = 0; j < 8; ++j) {
                const int c = 16 * (f & 1) + 8 * (j >> 2) + 4 * hi + (j & 3);
                if (kind == 0) e[i][j] = src[(size_t)(32 * (f >> 1) + c) * ldn + r];
                else {
                    const int ct = f >> 5, hd = (f >> 1) & 15; const bool pad = (c >= 24 && c < 28); const int d = c < 24 ? c : c - 4;
                    const unsigned short v = src[(size_t)(hd * DH + d) * ldn + ct * 32 + r];
                    e[i][j] = pad ? (unsigned short)0 : v;
                }
            }
        }
        __builtin_amdgcn_sched_barrier(0);
#pragma unroll
        for (int g = 0; g < 7; ++g)
#pragma unroll
            for (int sl = 0; sl < 3; ++sl)
                glds16(src0 + (size_t)g * 64 * (3 * DM * 2) + sl * (DM * 2), (unsigned)__builtin_amdgcn_readfirstlane(lds0 + K1_W + (sl * 28 + 4 * g + L) * 1024));
        float cs[3] = {0.f, 0.f, 0.f};
#define K1_STAGE(g, N) { asm volatile("s_waitcnt vmcnt(" #N ")\n\ts_barrier" ::: "memory"); \
            _Pragma("unroll") for (int sl = 0; sl < 3; ++sl) { const u32x4 f = trfrag(trp + (sl * 28 + 4 * g + L) * 1024); \
                cs[sl] = dot2(f.x, 0x3f803f80u, cs[sl]); cs[sl] = dot2(f.y, 0x3f803f80u, cs[sl]); cs[sl] = dot2(f.z, 0x3f803f80u, cs[sl]); cs[sl] = dot2(f.w, 0x3f803f80u, cs[sl]); } }
        K1_STAGE(0, 18) K1_STAGE(1, 15) K1_STAGE(2, 12) K1_STAGE(3, 9) K1_STAGE(4, 6) K1_STAGE(5, 3) K1_STAGE(6, 0)
#pragma unroll
        for (int sl = 0; sl < 3; ++sl) ((LAS float*)(lds + K1_CS))[(L * 3 + sl) * 64 + lane] = cs[sl];
        float c1 = 0.f;
#pragma unroll
        for (int i = 0; i < 4; ++i) {
            const u32x4 v = {e[i][0] | ((unsigned)e[i][1] << 16), e[i][2] | ((unsigned)e[i][3] << 16), e[i][4] | ((unsigned)e[i][5] << 16), e[i][6] | ((unsigned)e[i][7] << 16)};
            if (L + 4 * i < 14) {
                *(u32x4*)(dst + (size_t)(L + 4 * i) * KB + lane * 16) = v;
                c1 = dot2(v.x, 0x3f803f80u, c1); c1 = dot2(v.y, 0x3f803f80u, c1); c1 = dot2(v.z, 0x3f803f80u, c1); c1 = dot2(v.w, 0x3f803f80u, c1);
            }
        }
        ((LAS float*)(lds + K1_C1))[L * 64 + lane] = c1;
        BAR();
        if (b < 112 && w == 4 && lane < 32) {
            float s = 0.f;
#pragma unroll
            for (int i = 0; i < 8; ++i) s += ((LAS float*)(lds + K1_C1))[i * 32 + lane];
            ((float*)(ws + CS_OFF))[(b & 1) * FF + (b >> 1) * 32 + lane] = s;
        }
        return;
    }
    const int rt = rb * 4 + w;
    const unsigned short* xp = x + (size_t)(rt * 32 + r) * DM + 8 * hi;
    u32x4 xf[28];
#pragma unroll
    for (int ks = 0; ks < 28; ++ks) xf[ks] = *(const u32x4*)(xp + 16 * ks);
    f32x16 aq = {}, ak = {}, av = {};
    float s1 = 0.f, s2 = 0.f;
#pragma unroll
    for (int g = 0; g < 7; ++g) {
        asm volatile("s_barrier" ::: "memory");
#pragma unroll
        for (int i = 0; i < 4; ++i) {
            const int ks = 4 * g + i;
            const u32x4 fq = trfrag(trp + (0 * 28 + ks) * 1024), fk = trfrag(trp + (1 * 28 + ks) * 1024), fv = trfrag(trp + (2 * 28 + ks) * 1024);
            aq = MFMA(fq, xf[ks], aq); ak = MFMA(fk, xf[ks], ak); av = MFMA(xf[ks], fv, av);
            s1 = dot2(xf[ks].x, 0x3f803f80u, s1); s1 = dot2(xf[ks].y, 0x3f803f80u, s1); s1 = dot2(xf[ks].z, 0x3f803f80u, s1); s1 = dot2(xf[ks].w, 0x3f803f80u, s1);
            s2 = dot2(xf[ks].x, xf[ks].x, s2); s2 = dot2(xf[ks].y, xf[ks].y, s2); s2 = dot2(xf[ks].z, xf[ks].z, s2); s2 = dot2(xf[ks].w, xf[ks].w, s2);
        }
    }
    s1 = swap_add(s1); s2 = swap_add(s2);
    const float mu = s1 * (1.f / DM), var = fmaxf(s2 * (1.f / DM) - mu * mu, 0.f), rstd = 1.f / sqrtf(var + EPS);
    LAS float* st = (LAS float*)(lds + K1_ST) + w * 64;
    st[lane] = hi ? rstd : mu;
    BAR();
    LAS const float* csp = (LAS const float*)(lds + K1_CS);
    float cv = 0.f;
#pragma unroll
    for (int L = 0; L < 4; ++L) cv += csp[(L * 3 + 2) * 64 + r] + csp[(L * 3 + 2) * 64 + 32 + r];
    const float rq = rstd * QSCALE;
#pragma unroll
    for (int g = 0; g < 4; ++g) {
        f32x4 cq = {}, ck = {};
#pragma unroll
        for (int L = 0; L < 4; ++L)
#pragma unroll
            for (int hh = 0; hh < 2; ++hh) {
                cq += *(LAS const f32x4*)(csp + (L * 3 + 0) * 64 + hh * 32 + 8 * g + 4 * hi);
                ck += *(LAS const f32x4*)(csp + (L * 3 + 1) * 64 + hh * 32 + 8 * g + 4 * hi);
            }
        const f32x4 mt = *(LAS const f32x4*)(st + 8 * g + 4 * hi), rs = *(LAS const f32x4*)(st + 32 + 8 * g + 4 * hi);
#pragma unroll
        for (int i = 0; i < 4; ++i) {
            aq[4 * g + i] = (aq[4 * g + i] - mu * cq[i]) * rq;
            ak[4 * g + i] = (ak[4 * g + i] - mu * ck[i]) * rstd;
            av[4 * g + i] = (av[4 * g + i] - mt[i] * cv) * rs[i];
        }
    }
    u32x4 q0 = pack8(aq, 0), q1 = pack8(aq, 1), k0 = pack8(ak, 0), k1 = pack8(ak, 1), v0 = pack8(av, 0), v1 = pack8(av, 1);
    if (hi == 0) { q1.z = 0u; q1.w = 0u; k1.z = 0x3f803f80u; k1.w = 0x00003f80u; }
    if (r >= 24 && r < 28) { const unsigned o = (r == 24) ? 0x3f803f80u : 0u; v0 = u32x4{o, o, o, o}; v1 = v0; }
    f32x16 sd = {};
    sd = MFMA(k0, q0, sd); sd = MFMA(k1, q1, sd);
    const float m = swap_max(max16(sd));
    if (hi == 0) { const u32x2 p = split3(m); q1.z = p.x; q1.w = p.y; }
    char* qd = ws + QF_OFF + (size_t)(h * 64 + rt) * 2 * KB + lane * 16;
    char* kd = ws + KV_OFF + (size_t)(h * 64 + rt) * 4 * KB + lane * 16;
    *(u32x4*)(qd) = q0; *(u32x4*)(qd + KB) = q1;
    *(u32x4*)(kd) = k0; *(u32x4*)(kd + KB) = k1; *(u32x4*)(kd + 2 * KB) = v0; *(u32x4*)(kd + 3 * KB) = v1;
}

constexpr int K2_RED = 0, K2_MX = 64 * 1024, K2_FLAG = K2_MX + 8 * 2 * 32 * 4, K2_LDS = K2_FLAG + 64;
__global__ __launch_bounds__(512, 2) void k2_attn(char* __restrict__ ws) {
    extern __shared__ __attribute__((aligned(16))) char smem[];
    LAS char* lds = (LAS char*)smem;
    const int tid = threadIdx.x, lane = tid & 63, w = __builtin_amdgcn_readfirstlane(tid >> 6), r = lane & 31, hi = lane >> 5;
    const int b = blockIdx.x, xcd = b & 7, bi = b >> 3;
    const int h = 2 * xcd + (bi >> 4), rb = bi & 15;
    const char* qp = ws + QF_OFF + (size_t)(h * 64 + rb * 4) * 2 * KB + lane * 16;
    const char* kvp = ws + KV_OFF + (size_t)(h * 64 + w * 8) * 4 * KB + lane * 16;
    u32x4 qa0 = *(const u32x4*)(qp), qa1 = *(const u32x4*)(qp + KB), qb0 = *(const u32x4*)(qp + 2 * KB), qb1 = *(const u32x4*)(qp + 3 * KB);
    u32x4 kv[8][4];
#pragma unroll
    for (int t = 0; t < 8; ++t)
#pragma unroll
        for (int i = 0; i < 4; ++i) kv[t][i] = *(const u32x4*)(kvp + (size_t)(t * 4 + i) * KB);
    volatile LAS unsigned* flag = (volatile LAS unsigned*)(lds + K2_FLAG);
    if (tid == 0) { flag[0] = 0u; flag[1] = 0u; }
    BAR();
    for (int pass = 0; pass < 2; ++pass) {
        if (pass == 1) { qa0 = *(const u32x4*)(qp + 4 * KB); qa1 = *(const u32x4*)(qp + 5 * KB); qb0 = *(const u32x4*)(qp + 6 * KB); qb1 = *(const u32x4*)(qp + 7 * KB); }
        for (int attempt = 0;; ++attempt) {
            f32x16 oa = {}, ob = {};
#pragma unroll
            for (int t = 0; t < 8; ++t) {
                f32x16 sa = {}, sb = {};
                sa = MFMA(kv[t][0], qa0, sa); sa = MFMA(kv[t][1], qa1, sa);
                sb = MFMA(kv[t][0], qb0, sb); sb = MFMA(kv[t][1], qb1, sb);
#pragma unroll
                for (int i = 0; i < 16; ++i) sa[i] = __builtin_amdgcn_exp2f(sa[i]);
                oa = MFMA(kv[t][2], pack8(sa, 0), oa); oa = MFMA(kv[t][3], pack8(sa, 1), oa);
#pragma unroll
                for (int i = 0; i < 16; ++i) sb[i] = __builtin_amdgcn_exp2f(sb[i]);
                ob = MFMA(kv[t][2], pack8(sb, 0), ob); ob = MFMA(kv[t][3], pack8(sb, 1), ob);
            }
            const bool bad = (hi == 0) && !(oa[12] < 1.0e18f && ob[12] < 1.0e18f);
            if (attempt == 0 && __any(bad) && lane == 0) flag[pass] = 1u;
            LAS f32x4* red = (LAS f32x4*)(lds + K2_RED) + (w * 8) * 64 + lane;
#pragma unroll
            for (int g = 0; g < 4; ++g) {
                red[g * 64] = f32x4{oa[4 * g], oa[4 * g + 1], oa[4 * g + 2], oa[4 * g + 3]};
                red[(4 + g) * 64] = f32x4{ob[4 * g], ob[4 * g + 1], ob[4 * g + 2], ob[4 * g + 3]};
            }
            BAR();
            if (attempt == 0 && flag[pass] != 0u) {
                float ma = -INFINITY, mb = -INFINITY;
#pragma unroll 1
                for (int t = 0; t < 8; ++t) {
                    const u32x4 k0 = *(const u32x4*)(kvp + (size_t)(t * 4) * KB), k1 = *(const u32x4*)(kvp + (size_t)(t * 4 + 1) * KB);
                    f32x16 sa = {}, sb = {};
                    sa = MFMA(k0, qa0, sa); sa = MFMA(k1, qa1, sa);
                    sb = MFMA(k0, qb0, sb); sb = MFMA(k1, qb1, sb);
                    ma = fmaxf(ma, max16(sa)); mb = fmaxf(mb, max16(sb));
                }
                ma = swap_max(ma); mb = swap_max(mb);
                LAS float* mx = (LAS float*)(lds + K2_MX);
                mx[w * 64 + lane] = hi ? mb : ma;
                BAR();
#pragma unroll
                for (int i = 0; i < 8; ++i) { ma = fmaxf(ma, mx[i * 64 + r]); mb = fmaxf(mb, mx[i * 64 + 32 + r]); }
                if (hi == 0) {
                    const float oma = -(bflo(qa1.z) + bfhi(qa1.z) + bflo(qa1.w)), omb = -(bflo(qb1.z) + bfhi(qb1.z) + bflo(qb1.w));
                    const u32x2 pa = split3(oma + ma), pb = split3(omb + mb);
                    qa1.z = pa.x; qa1.w = pa.y; qb1.z = pb.x; qb1.w = pb.y;
                }
                BAR();
                continue;
            }
            break;
        }
        {
            LAS const f32x4* red = (LAS const f32x4*)(lds + K2_RED) + w * 64 + lane;
            LAS const float* lp = (LAS const float*)(lds + K2_RED) + (((w >> 2) * 4 + 3) * 64 + r) * 4;
            f32x4 o = {}; float l = 0.f;
#pragma unroll
            for (int i = 0; i < 8; ++i) { o += red[i * 8 * 64]; l += lp[i * 8 * 64 * 4]; }
            const float il = 1.f / l;
            const int qt = rb * 4 + pass * 2 + (w >> 2), g = w & 3;
            char* od = ws + OF_OFF + (size_t)((qt * 16 + h) * 2 + (g >> 1)) * KB + lane * 16 + (g & 1) * 8;
            *(u32x2*)od = u32x2{pk2(o[0] * il, o[1] * il), pk2(o[2] * il, o[3] * il)};
        }
        BAR();
    }
}

constexpr int K3_LDS = 8 * 2 * 1024;
__global__ __launch_bounds__(512, 2) void k3_wo(const unsigned short* __restrict__ x, char* __restrict__ ws) {
    extern __shared__ __attribute__((aligned(16))) char smem[];
    LAS char* lds = (LAS char*)smem;
    const int tid = threadIdx.x, lane = tid & 63, w = __builtin_amdgcn_readfirstlane(tid >> 6), r = lane & 31, hi = lane >> 5;
    const int b = blockIdx.x, xcd = b & 7, bi = b >> 3;
    const int rb = 2 * xcd + (bi >= 14), ct = bi >= 14 ? bi - 14 : bi;
    const int rt = rb * 4 + (w & 3), kh = w >> 2;
    const char* op = ws + OF_OFF + (size_t)(rt * 32 + kh * 16) * KB + lane * 16;
    const char* wp = ws + WO_OFF + (size_t)(ct * 32 + kh * 16) * KB + lane * 16;
    u32x4 of[16], wf[16];
#pragma unroll
    for (int i = 0; i < 16; ++i) { of[i] = *(const u32x4*)(op + i * KB); wf[i] = *(const u32x4*)(wp + i * KB); }
    const unsigned short* xp = x + (size_t)(rt * 32 + r) * DM + ct * 32 + 16 * kh + 4 * hi;
    const u32x2 x0 = *(const u32x2*)xp, x1 = *(const u32x2*)(xp + 8);
    __builtin_amdgcn_sched_barrier(0);
    f32x16 acc = {};
#pragma unroll
    for (int i = 0; i < 16; ++i) acc = MFMA(wf[i], of[i], acc);
    LAS f32x4* red = (LAS f32x4*)lds;
    const int o = 8 * (1 - kh);
    f32x4 g0, g1, m0, m1;
    if (kh) { g0 = f32x4{acc[0], acc[1], acc[2], acc[3]}; g1 = f32x4{acc[4], acc[5], acc[6], acc[7]}; m0 = f32x4{acc[8], acc[9], acc[10], acc[11]}; m1 = f32x4{acc[12], acc[13], acc[14], acc[15]}; }
    else { m0 = f32x4{acc[0], acc[1], acc[2], acc[3]}; m1 = f32x4{acc[4], acc[5], acc[6], acc[7]}; g0 = f32x4{acc[8], acc[9], acc[10], acc[11]}; g1 = f32x4{acc[12], acc[13], acc[14], acc[15]}; }
    (void)o;
    red[(w * 2) * 64 + lane] = g0; red[(w * 2 + 1) * 64 + lane] = g1;
    BAR();
    m0 += red[((w ^ 4) * 2) * 64 + lane]; m1 += red[((w ^ 4) * 2 + 1) * 64 + lane];
    const u32x4 hv = {pk2(m0[0] + bflo(x0.x), m0[1] + bfhi(x0.x)), pk2(m0[2] + bflo(x0.y), m0[3] + bfhi(x0.y)),
                      pk2(m1[0] + bflo(x1.x), m1[1] + bfhi(x1.x)), pk2(m1[2] + bflo(x1.y), m1[3] + bfhi(x1.y))};
    *(u32x4*)(ws + HF_OFF + (size_t)(rt * 28 + ct * 2 + kh) * KB + lane * 16) = hv;
}

constexpr int K4_W = 0, K4_ST = 112 * 1024, K4_LDS = K4_ST + 8 * 64 * 8;
__global__ __launch_bounds__(512, 2) void k4_ffn1(char* __restrict__ ws) {
    extern __shared__ __attribute__((aligned(16))) char smem[];
    LAS char* lds = (LAS char*)smem;
    const int tid = threadIdx.x, lane = tid & 63, w = __builtin_amdgcn_readfirstlane(tid >> 6), r = lane & 31, hi = lane >> 5;
    const int b = blockIdx.x, xcd = b & 7, bi = b >> 3;
    const int rb = 2 * xcd + (bi >= 14), fg = bi >= 14 ? bi - 14 : bi;
    const int rt = rb * 4 + (w & 3), kh = w >> 2;
    const unsigned lds0 = (unsigned)(uintptr_t)smem;
    const char* wsrc = ws + W1_OFF + (size_t)(fg * 4 * 28 + w * 14) * KB + lane * 16;
#pragma unroll
    for (int i = 0; i < 14; ++i) glds16(wsrc + i * KB, (unsigned)__builtin_amdgcn_readfirstlane(lds0 + K4_W + (w * 14 + i) * 1024));
    const char* hp = ws + HF_OFF + (size_t)(rt * 28 + kh * 14) * KB + lane * 16;
    u32x4 hf[14];
#pragma unroll
    for (int i = 0; i < 14; ++i) hf[i] = *(const u32x4*)(hp + i * KB);
    f32x4 cs[2][4];
#pragma unroll
    for (int t = 0; t < 2; ++t)
#pragma unroll
        for (int g = 0; g < 4; ++g) {
            const float* cp = (const float*)(ws + CS_OFF) + (fg * 4 + 2 * kh + t) * 32 + 8 * g + 4 * hi;
            cs[t][g] = *(const f32x4*)cp + *(const f32x4*)(cp + FF);
        }
    asm volatile("s_waitcnt vmcnt(0)\n\ts_barrier" ::: "memory");
    f32x16 acc[4] = {};
    float s1 = 0.f, s2 = 0.f;
    LAS const u32x4* wl = (LAS const u32x4*)(lds + K4_W) + (kh * 14) * 64 + lane;
#pragma unroll
    for (int i = 0; i < 14; ++i) {
#pragma unroll
        for (int t = 0; t < 4; ++t) acc[t] = MFMA(wl[(t * 28 + i) * 64], hf[i], acc[t]);
        s1 = dot2(hf[i].x, 0x3f803f80u, s1); s1 = dot2(hf[i].y, 0x3f803f80u, s1); s1 = dot2(hf[i].z, 0x3f803f80u, s1); s1 = dot2(hf[i].w, 0x3f803f80u, s1);
        s2 = dot2(hf[i].x, hf[i].x, s2); s2 = dot2(hf[i].y, hf[i].y, s2); s2 = dot2(hf[i].z, hf[i].z, s2); s2 = dot2(hf[i].w, hf[i].w, s2);
    }
    s1 = swap_add(s1); s2 = swap_add(s2);
    BAR();
    LAS f32x4* red = (LAS f32x4*)lds;
    f32x16 m0, m1, g0, g1;
    if (kh) { g0 = acc[0]; g1 = acc[1]; m0 = acc[2]; m1 = acc[3]; } else { m0 = acc[0]; m1 = acc[1]; g0 = acc[2]; g1 = acc[3]; }
#pragma unroll
    for (int g = 0; g < 4; ++g) {
        red[(w * 8 + g) * 64 + lane] = f32x4{g0[4 * g], g0[4 * g + 1], g0[4 * g + 2], g0[4 * g + 3]};
        red[(w * 8 + 4 + g) * 64 + lane] = f32x4{g1[4 * g], g1[4 * g + 1], g1[4 * g + 2], g1[4 * g + 3]};
    }
    ((LAS f32x2*)(lds + K4_ST))[w * 64 + lane] = f32x2{s1, s2};
    BAR();
    const f32x2 so = ((LAS f32x2*)(lds + K4_ST))[(w ^ 4) * 64 + lane];
    s1 += so.x; s2 += so.y;
    const float mu = s1 * (1.f / DM), var = fmaxf(s2 * (1.f / DM) - mu * mu, 0.f), rstd = 1.f / sqrtf(var + EPS);
#pragma unroll
    for (int g = 0; g < 4; ++g) {
        const f32x4 a = red[((w ^ 4) * 8 + g) * 64 + lane], c = red[((w ^ 4) * 8 + 4 + g) * 64 + lane];
#pragma unroll
        for (int i = 0; i < 4; ++i) {
            const float z0 = (m0[4 * g + i] + a[i] - mu * cs[0][g][i]) * rstd, z1 = (m1[4 * g + i] + c[i] - mu * cs[1][g][i]) * rstd;
            m0[4 * g + i] = z0 * __builtin_amdgcn_rcpf(1.f + __builtin_amdgcn_exp2f(-1.4426950408889634f * z0));
            m1[4 * g + i] = z1 * __builtin_amdgcn_rcpf(1.f + __builtin_amdgcn_exp2f(-1.4426950408889634f * z1));
        }
    }
    char* ud = ws + UF_OFF + (size_t)(rt * 112 + (fg * 4 + 2 * kh) * 2) * KB + lane * 16;
    *(u32x4*)(ud) = pack8(m0, 0); *(u32x4*)(ud + KB) = pack8(m0, 1); *(u32x4*)(ud + 2 * KB) = pack8(m1, 0); *(u32x4*)(ud + 3 * KB) = pack8(m1, 1);
}

constexpr int K5_LDS = 128 * 1024;
__global__ __launch_bounds__(512, 2) void k5_ffn2(char* __restrict__ ws, unsigned short* __restrict__ out) {
    extern __shared__ __attribute__((aligned(16))) char smem[];
    LAS char* lds = (LAS char*)smem;
    const int tid = threadIdx.x, lane = tid & 63, w = __builtin_amdgcn_readfirstlane(tid >> 6), r = lane & 31, hi = lane >> 5;
    const int b = blockIdx.x, xcd = b & 7, bi = b >> 3;
    const int rp = 4 * xcd + bi / 7, cp = bi % 7;
    const char* up = ws + UF_OFF + (size_t)(rp * 2 * 112 + w * 14) * KB + lane * 16;
    const char* wp = ws + W2_OFF + (size_t)(cp * 2 * 112 + w * 14) * KB + lane * 16;
    u32x4 uf[2][14], wf[2][14];
#define K5_LOAD(i) { uf[0][i] = *(const u32x4*)(up + (i) * KB); wf[0][i] = *(const u32x4*)(wp + (i) * KB); \
        uf[1][i] = *(const u32x4*)(up + (112 + (i)) * KB); wf[1][i] = *(const u32x4*)(wp + (112 + (i)) * KB); }
#pragma unroll
    for (int i = 0; i < 10; ++i) K5_LOAD(i)
    const int rt = rp * 2 + (w >> 2), ct = cp * 2 + ((w >> 1) & 1), s = w & 1;
    const u32x4 hv = *(const u32x4*)(ws + HF_OFF + (size_t)(rt * 28 + ct * 2 + s) * KB + lane * 16);
    __builtin_amdgcn_sched_barrier(0);
    f32x16 acc[4] = {};
#pragma unroll
    for (int i = 0; i < 14; ++i) {
        acc[0] = MFMA(wf[0][i], uf[0][i], acc[0]); acc[1] = MFMA(wf[1][i], uf[0][i], acc[1]);
        acc[2] = MFMA(wf[0][i], uf[1][i], acc[2]); acc[3] = MFMA(wf[1][i], uf[1][i], acc[3]);
        if (i + 10 < 14) { __builtin_amdgcn_sched_barrier(0); K5_LOAD(i + 10) __builtin_amdgcn_sched_barrier(0); }
    }
    LAS f32x4* red = (LAS f32x4*)lds;
#pragma unroll
    for (int t = 0; t < 4; ++t)
#pragma unroll
        for (int g = 0; g < 4; ++g) red[(w * 16 + t * 4 + g) * 64 + lane] = f32x4{acc[t][4 * g], acc[t][4 * g + 1], acc[t][4 * g + 2], acc[t][4 * g + 3]};
    BAR();
    f32x4 m0 = {}, m1 = {};
#pragma unroll
    for (int i = 0; i < 8; ++i) { m0 += red[(i * 16 + w * 2) * 64 + lane]; m1 += red[(i * 16 + w * 2 + 1) * 64 + lane]; }
    unsigned a0 = pk2(m0[0] + bflo(hv.x), m0[1] + bfhi(hv.x)), a1 = pk2(m0[2] + bflo(hv.y), m0[3] + bfhi(hv.y));
    unsigned b0 = pk2(m1[0] + bflo(hv.z), m1[1] + bfhi(hv.z)), b1 = pk2(m1[2] + bflo(hv.w), m1[3] + bfhi(hv.w));
    auto r0 = __builtin_amdgcn_permlane32_swap(a0, b0, false, false); auto r1 = __builtin_amdgcn_permlane32_swap(a1, b1, false, false);
    *(u32x4*)(out + (size_t)(rt * 32 + r) * DM + ct * 32 + 16 * s + 8 * hi) = u32x4{r0[0], r1[0], r0[1], r1[1]};
}

extern "C" void kernel_launch(void* const* d_in, const int* in_sizes, int n_in, void* d_out, int out_size, void* d_ws, size_t ws_size, hipStream_t stream) {
    const unsigned short* x = (const unsigned short*)d_in[0];
    const unsigned short* wqkv = (const unsigned short*)d_in[1];
    const unsigned short* wo = (const unsigned short*)d_in[2];
    const unsigned short* w1 = (const unsigned short*)d_in[4];
    const unsigned short* w2 = (const unsigned short*)d_in[5];
    char* ws = (char*)d_ws;
    (void)hipFuncSetAttribute((const void*)k1_qkv, hipFuncAttributeMaxDynamicSharedMemorySize, K1_LDS);
    (void)hipFuncSetAttribute((const void*)k2_attn, hipFuncAttributeMaxDynamicSharedMemorySize, K2_LDS);
    (void)hipFuncSetAttribute((const void*)k4_ffn1, hipFuncAttributeMaxDynamicSharedMemorySize, K4_LDS);
    (void)hipFuncSetAttribute((const void*)k5_ffn2, hipFuncAttributeMaxDynamicSharedMemorySize, K5_LDS);
    k1_qkv<<<256, 512, K1_LDS, stream>>>(x, wqkv, wo, w1, w2, ws);
    k2_attn<<<256, 512, K2_LDS, stream>>>(ws);
    k3_wo<<<224, 512, K3_LDS, stream>>>(x, ws);
    k4_ffn1<<<224, 512, K4_LDS, stream>>>(ws);
    k5_ffn2<<<224, 512, K5_LDS, stream>>>(ws, (unsigned short*)d_out);
}
```

```cpp
#include <hip/hip_runtime.h>
#include <hip/hip_bf16.h>
#include <stdint.h>

typedef short bf16x8 __attribute__((ext_vector_type(8)));
typedef float f32x16 __attribute__((ext_vector_type(16)));
typedef float f32x4 __attribute__((ext_vector_type(4)));
typedef float f32x2 __attribute__((ext_vector_type(2)));
typedef unsigned u32x4 __attribute__((ext_vector_type(4)));
typedef unsigned u32x2 __attribute__((ext_vector_type(2)));
typedef __bf16 bf16x2_t __attribute__((ext_vector_type(2)));
typedef short v4i16_t __attribute__((ext_vector_type(4)));
#define LAS __attribute__((address_space(3)))
#define GAS __attribute__((address_space(1)))
#define DI __device__ __forceinline__
#define MFMA(a, b, c) __builtin_amdgcn_mfma_f32_32x32x16_bf16(__builtin_bit_cast(bf16x8, (a)), __builtin_bit_cast(bf16x8, (b)), (c), 0, 0, 0)
#define BAR() asm volatile("s_waitcnt lgkmcnt(0)\n\ts_barrier" ::: "memory")
#define DRAIN_BAR() asm volatile("s_waitcnt vmcnt(0) lgkmcnt(0)\n\ts_barrier" ::: "memory")
#define VMBAR(N) asm volatile("s_waitcnt vmcnt(" #N ") lgkmcnt(0)\n\ts_barrier" ::: "memory")
#define VMWAIT(N) asm volatile("s_waitcnt vmcnt(" #N ")" ::: "memory")
#define SB() __builtin_amdgcn_sched_barrier(0)
#define RLX_AGENT __ATOMIC_RELAXED, __HIP_MEMORY_SCOPE_AGENT

constexpr int SEQ = 2048, DM = 448, NH = 16, DH = 28, FF = 1792;
constexpr float EPS = 1e-5f;
constexpr float LOG2E = 1.4426950408889634f;
constexpr float QSCALE = 0.18898223650461362f * LOG2E;
constexpr unsigned ONES = 0x3f803f80u;
constexpr int KB = 1024;
constexpr int FL_OFF = 0;
constexpr int QF_OFF = 32 * KB;
constexpr int KV_OFF = QF_OFF + 16 * 64 * 2 * KB;
constexpr int OF_OFF = KV_OFF + 16 * 64 * 4 * KB;
constexpr int WO_OFF = OF_OFF + 64 * 32 * KB;
constexpr int HF_OFF = WO_OFF + 14 * 32 * KB;
constexpr int W1_OFF = HF_OFF + 64 * 28 * KB;
constexpr int UF_OFF = W1_OFF + 56 * 28 * KB;
constexpr int W2_OFF = UF_OFF + 64 * 112 * KB;
constexpr int WS_BYTES = W2_OFF + 14 * 112 * KB;
constexpr int SC1 = 16;

DI unsigned pk2(float lo, float hi) { f32x2 v = {lo, hi}; return __builtin_bit_cast(unsigned, __builtin_convertvector(v, bf16x2_t)); }
DI float dot2(unsigned a, unsigned b, float c) { return __builtin_amdgcn_fdot2_f32_bf16(__builtin_bit_cast(bf16x2_t, a), __builtin_bit_cast(bf16x2_t, b), c, false); }
DI float sum8(const u32x4& f, float c) { return dot2(f.w, ONES, dot2(f.z, ONES, dot2(f.y, ONES, dot2(f.x, ONES, c)))); }
DI float sq8(const u32x4& f, float c) { return dot2(f.w, f.w, dot2(f.z, f.z, dot2(f.y, f.y, dot2(f.x, f.x, c)))); }
DI float bflo(unsigned u) { return __uint_as_float(u << 16); }
DI float bfhi(unsigned u) { return __uint_as_float(u & 0xffff0000u); }
DI float swap_add(float v) { auto r = __builtin_amdgcn_permlane32_swap(__float_as_uint(v), __float_as_uint(v), false, false); return __uint_as_float(r[0]) + __uint_as_float(r[1]); }
DI float swap_max(float v) { auto r = __builtin_amdgcn_permlane32_swap(__float_as_uint(v), __float_as_uint(v), false, false); return fmaxf(__uint_as_float(r[0]), __uint_as_float(r[1])); }
DI void glds16(const void* sbase, unsigned voff, unsigned lds_base) {
    unsigned sv;
    asm volatile("s_mov_b32 %0, m0\n\ts_mov_b32 m0, %3\n\ts_nop 0\n\tglobal_load_lds_dwordx4 %1, %2\n\ts_mov_b32 m0, %0" : "=&s"(sv) : "v"(voff), "s"(sbase), "s"(lds_base) : "memory");
}
DI u32x2 trd(LAS const char* p) { return __builtin_bit_cast(u32x2, __builtin_amdgcn_ds_read_tr16_b64_v4i16((LAS v4i16_t*)p)); }
DI u32x4 trfrag(LAS const char* p, int second) { u32x2 a = trd(p), b = trd(p + second); return u32x4{a.x, a.y, b.x, b.y}; }
DI u32x4 pack8(const f32x16& a, int s) {
    return u32x4{pk2(a[8 * s], a[8 * s + 1]), pk2(a[8 * s + 2], a[8 * s + 3]), pk2(a[8 * s + 4], a[8 * s + 5]), pk2(a[8 * s + 6], a[8 * s + 7])};
}
DI float max16(const f32x16& p) {
    float a = fmaxf(fmaxf(p[0], p[1]), p[2]), b = fmaxf(fmaxf(p[3], p[4]), p[5]);
    a = fmaxf(fmaxf(a, p[6]), p[7]); b = fmaxf(fmaxf(b, p[8]), p[9]);
    a = fmaxf(fmaxf(a, p[10]), p[11]); b = fmaxf(fmaxf(b, p[12]), p[13]);
    a = fmaxf(fmaxf(a, p[14]), p[15]);
    return fmaxf(a, b);
}
DI u32x2 split3(float m) {
    const float v = -m;
    const unsigned u1 = __float_as_uint(v) & 0xffff0000u; const float r1 = v - __uint_as_float(u1);
    const unsigned u2 = __float_as_uint(r1) & 0xffff0000u; const float r2 = r1 - __uint_as_float(u2);
    const unsigned u3 = __float_as_uint(r2) & 0xffff0000u;
    return u32x2{(u1 >> 16) | u2, u3 >> 16};
}
DI u32x4 corr_a(float cs, int hi) {
    const unsigned h = pk2(cs, cs); const float lo = cs - bflo(h); const unsigned l = pk2(lo, lo);
    return hi ? u32x4{0u, 0u, 0u, 0u} : u32x4{h, l, 0u, 0u};
}
DI u32x4 corr_b(float mu) {
    const unsigned h = pk2(-mu, 0.f); const unsigned d = pk2(-mu, -mu - bflo(h));
    return u32x4{d, d, 0u, 0u};
}
DI u32x4 ldf(__amdgpu_buffer_rsrc_t rs, int l16, int soff) { return __builtin_amdgcn_raw_buffer_load_b128(rs, l16, soff, 0); }
DI u32x4 ldf1(__amdgpu_buffer_rsrc_t rs, int l16, int soff) { return __builtin_amdgcn_raw_buffer_load_b128(rs, l16, soff, SC1); }

#define STAMP(kid, i)
#define STAMP_FLUSH(k0, nk)
constexpr int STAMP_BYTES = 0;

constexpr int A_SLOT = 24 * KB;
constexpr int A_WQ = 1 * A_SLOT, A_PT = A_WQ + 28 * KB + 64;
constexpr int A_CS = 6 * A_SLOT;
constexpr int A_LDS = A_CS + 3 * KB + STAMP_BYTES;
__global__ __launch_bounds__(512, 2) void kA(const unsigned short* __restrict__ x, const unsigned short* __restrict__ wqkv, const unsigned short* __restrict__ wo,
                                             const unsigned short* __restrict__ w1, const unsigned short* __restrict__ w2, char* __restrict__ ws) {
    extern __shared__ __attribute__((aligned(16))) char smem[];
    LAS char* lds = (LAS char*)smem;
    constexpr int STAMP_LDS = A_CS + 3 * KB;
    const unsigned lds0 = (unsigned)(uintptr_t)smem;
    const int tid = threadIdx.x, lane = tid & 63, w = __builtin_amdgcn_readfirstlane(tid >> 6), r = lane & 31, hi = lane >> 5;
    const int b = blockIdx.x, xcd = b & 7, bi = b >> 3;
    const int j = (xcd & 3) * 4 + (bi & 3), rb = (xcd >> 2) * 8 + (bi >> 2);
    STAMP(0, 0);
    const int trq = (lane & 15) >> 2, trc = ((lane >> 4) & 1) * 32 + (lane & 3) * 8;
    const __amdgpu_buffer_rsrc_t rs = __builtin_amdgcn_make_buffer_rsrc((void*)ws, 0, WS_BYTES, 0x00020000);
    const int l16 = lane * 16;
    LAS float* csl = (LAS float*)(lds + A_CS);
    if (w >= 4) {
        const int L = w - 4;
        const char* wkv = (const char*)wqkv + (size_t)(16 * L) * 2688 + 896 + 112 * j;
        const unsigned woff = (lane >> 3) * 2688 + ((lane & 7) < 7 ? (lane & 7) : 6) * 16;
#define A_WLOAD(s, slot) { glds16(wkv + (size_t)(s) * 64 * 2688, woff, lds0 + (slot) * A_SLOT + 16 * KB + L * 2048); \
                           glds16(wkv + (size_t)(s) * 64 * 2688 + 8 * 2688, woff, lds0 + (slot) * A_SLOT + 16 * KB + L * 2048 + 1024); }
#pragma unroll
        for (int s = 0; s < 6; ++s) A_WLOAD(s, s)
        LAS const char* tw = lds + 16 * KB + L * 2048 + (8 * hi + trq) * 128 + trc;
        float c0 = 0.f, c1 = 0.f;
#define A_CSUM(slot) { c0 = sum8(trfrag(tw + (slot) * A_SLOT, 512), c0); c1 = sum8(trfrag(tw + (slot) * A_SLOT + 56, 512), c1); }
        VMBAR(10); A_CSUM(0)
        VMBAR(8); A_WLOAD(6, 0) A_CSUM(1)
        VMBAR(8); A_CSUM(2)
        VMBAR(6);
        {
            const char* wq = (const char*)wqkv + (size_t)(16 * L) * 2688 + ((56 * j) & ~15);
            const unsigned qoff = (lane >> 2) * 2688 + (lane & 3) * 16;
#pragma unroll
            for (int i = 0; i < 7; ++i) glds16(wq + (size_t)i * 64 * 2688, qoff, lds0 + A_WQ + (4 * i + L) * 1024);
        }
        const char* psrc; int ppitch, pdst, pstep, k0;
        if (b < 112) { const int cp = b >> 2; k0 = (b & 3) * 7; psrc = (const char*)w1 + cp * 128; ppitch = FF * 2; pdst = W1_OFF + (cp * 2 * 28) * KB; pstep = 28 * KB; }
        else if (b < 224) { const int q = b - 112, cp = q >> 4; k0 = (q & 15) * 7; psrc = (const char*)w2 + cp * 128; ppitch = DM * 2; pdst = W2_OFF + (cp * 2 * 112) * KB; pstep = 112 * KB; }
        else { k0 = (b - 224) * 7; psrc = (const char*)wo; ppitch = DM * 2; pdst = WO_OFF; pstep = 32 * KB; }
        int pks[2], pd[2];
#pragma unroll
        for (int i = 0; i < 2; ++i) {
            const int kk = k0 + ((L + 4 * i < 7) ? L + 4 * i : L);
#pragma unroll
            for (int u = 0; u < 2; ++u) {
                int row = 8 * u + (lane >> 3), coff = 0;
                if (b < 224) { row += 16 * kk; pks[i] = 0; pd[i] = pdst + kk * KB; }
                else { const int cp = kk >> 5, ks = kk & 31, c = 16 * (ks & 1) + row; row = (ks >> 1) * DH + (c < DH ? c : DH - 1); coff = cp * 128; pks[i] = ks & 1; pd[i] = pdst + (cp * 64 + ks) * KB; }
                glds16(psrc, (unsigned)(row * ppitch + coff + (lane & 7) * 16), lds0 + A_PT + ((L * 2 + i) * 2 + u) * 1024);
            }
        }
        A_CSUM(3)
        VMBAR(15); A_CSUM(4)
        VMBAR(13); A_CSUM(5)
        VMBAR(11); A_CSUM(0)
        csl[(0 * 32 + r) * 8 + 2 * L + hi] = c0; csl[(1 * 32 + r) * 8 + 2 * L + hi] = c1;
        BAR();
        VMWAIT(4);
        float cq = 0.f;
        LAS const char* tq = lds + A_WQ + L * 1024 + (8 * hi + trq) * 64 + trc + ((56 * j) & 15);
#pragma unroll
        for (int i = 0; i < 7; ++i) cq = sum8(trfrag(tq + i * 4096, 256), cq);
        csl[(2 * 32 + r) * 8 + 2 * L + hi] = cq;
        BAR();
        VMWAIT(0);
        LAS const char* tp = lds + A_PT + L * 4096 + (4 * hi + trq) * 128 + trc;
#pragma unroll
        for (int i = 0; i < 2; ++i) {
            if (L + 4 * i < 7) {
#pragma unroll
                for (int ct = 0; ct < 2; ++ct) {
                    u32x4 f = trfrag(tp + i * 2048 + ct * 64, 1024);
                    if (b >= 224 && pks[i] && hi) { f.z = 0u; f.w = 0u; }
                    __builtin_amdgcn_raw_buffer_store_b128(f, rs, l16, pd[i] + ct * pstep, 0);
                }
            }
        }
        if (b < 3 * 64 && tid >= 256 && tid < 256 + 32) ((unsigned*)(ws + FL_OFF))[b * 32 + (tid - 256)] = 0u;
        STAMP(0, 6); STAMP_FLUSH(0, 1);
        return;
    }
    const int rt = rb * 4 + w;
    {
        const char* xb = (const char*)x + (size_t)(rt * 32) * 896;
        const unsigned xo0 = (lane >> 3) * 896 + (((lane & 7) ^ ((lane >> 4) & 7)) * 16), xo1 = (lane >> 3) * 896 + (((lane & 7) ^ (((lane >> 4) + 4) & 7)) * 16);
#define A_XLOAD(s, slot) { _Pragma("unroll") for (int oo = 0; oo < 4; ++oo) glds16(xb + oo * 8 * 896 + (s) * 128, (oo & 1) ? xo1 : xo0, lds0 + (slot) * A_SLOT + (4 * w + oo) * 1024); }
#pragma unroll
        for (int s = 0; s < 6; ++s) A_XLOAD(s, s)
        STAMP(0, 1);
        LAS const char* xr = lds + (4 * w + (r >> 3)) * 1024 + (r & 7) * 128;
        const int fx = (r >> 1) & 7;
        LAS const char* tw = lds + 16 * KB + (8 * hi + trq) * 128 + trc;
        u32x4 xf[28];
        f32x16 a0 = {}, a1 = {};
        float s1 = 0.f, s2 = 0.f;
#define A_STAGE(s, slot, N) { asm volatile("s_barrier\n\ts_waitcnt vmcnt(" #N ")" ::: "memory"); \
        _Pragma("unroll") for (int i = 0; i < 4; ++i) { \
            xf[4 * (s) + i] = *(LAS const u32x4*)(xr + (slot) * A_SLOT + (((2 * i + hi) ^ fx) * 16)); \
            a0 = MFMA(trfrag(tw + (slot) * A_SLOT + i * 2048, 512), xf[4 * (s) + i], a0); a1 = MFMA(trfrag(tw + (slot) * A_SLOT + i * 2048 + 56, 512), xf[4 * (s) + i], a1); \
            s1 = sum8(xf[4 * (s) + i], s1); s2 = sq8(xf[4 * (s) + i], s2); } \
        asm volatile("s_waitcnt lgkmcnt(0)" ::: "memory"); }
        A_STAGE(0, 0, 20) A_XLOAD(6, 0)
        STAMP(0, 2);
        A_STAGE(1, 1, 20) A_STAGE(2, 2, 16) A_STAGE(3, 3, 12) A_STAGE(4, 4, 8) A_STAGE(5, 5, 4) A_STAGE(6, 0, 0)
        STAMP(0, 3);
        s1 = swap_add(s1); s2 = swap_add(s2);
        const float mu = s1 * (1.f / DM), var = fmaxf(s2 * (1.f / DM) - mu * mu, 0.f), rstd = __builtin_amdgcn_rsqf(var + EPS);
        const u32x4 cb = corr_b(mu);
        BAR();
#define A_CS(t) ({ const f32x4 p0 = *(LAS const f32x4*)(csl + ((t) * 32 + r) * 8), p1 = *(LAS const f32x4*)(csl + ((t) * 32 + r) * 8 + 4); ((p0.x + p0.y) + (p0.z + p0.w)) + ((p1.x + p1.y) + (p1.z + p1.w)); })
        a0 = MFMA(corr_a(A_CS(0), hi), cb, a0); a1 = MFMA(corr_a(A_CS(1), hi), cb, a1);
        a0 *= rstd; a1 *= rstd;
        u32x4 f00 = pack8(a0, 0), f01 = pack8(a0, 1), f10 = pack8(a1, 0), f11 = pack8(a1, 1);
        const int hd = (2 * j) & 15;
        int dst = KV_OFF + (hd * 64 + rt) * 4 * KB;
        if (j < 8) {
            if (hi) { f01.z = ONES; f01.w = 0x00003f80u; f11.z = ONES; f11.w = 0x00003f80u; }
        } else {
            if (hi) { f01.z = 0x00003f80u; f01.w = 0u; f11.z = 0x00003f80u; f11.w = 0u; }
            LAS char* tb = lds + 3 * A_SLOT + w * 4096;
            LAS char* wr = tb + r * 64 + hi * 8;
            *(LAS u32x2*)(wr) = u32x2{f00.x, f00.y}; *(LAS u32x2*)(wr + 16) = u32x2{f00.z, f00.w}; *(LAS u32x2*)(wr + 32) = u32x2{f01.x, f01.y}; *(LAS u32x2*)(wr + 48) = u32x2{f01.z, f01.w};
            *(LAS u32x2*)(wr + 2048) = u32x2{f10.x, f10.y}; *(LAS u32x2*)(wr + 2064) = u32x2{f10.z, f10.w}; *(LAS u32x2*)(wr + 2080) = u32x2{f11.x, f11.y}; *(LAS u32x2*)(wr + 2096) = u32x2{f11.z, f11.w};
            asm volatile("s_waitcnt lgkmcnt(0)" ::: "memory");
            LAS const char* rd = tb + (4 * hi + trq) * 64 + trc;
            f00 = trfrag(rd, 512); f01 = trfrag(rd + 1024, 512); f10 = trfrag(rd + 2048, 512); f11 = trfrag(rd + 3072, 512);
            dst += 2 * KB;
        }
        __builtin_amdgcn_raw_buffer_store_b128(f00, rs, l16, dst, 0); __builtin_amdgcn_raw_buffer_store_b128(f01, rs, l16, dst + KB, 0);
        __builtin_amdgcn_raw_buffer_store_b128(f10, rs, l16, dst + 64 * 4 * KB, 0); __builtin_amdgcn_raw_buffer_store_b128(f11, rs, l16, dst + 64 * 4 * KB + KB, 0);
        STAMP(0, 4);
        BAR();
        LAS const char* tq = lds + A_WQ + (8 * hi + trq) * 64 + trc + ((56 * j) & 15);
        f32x16 aq = {};
#pragma unroll
        for (int ks = 0; ks < 28; ++ks) aq = MFMA(trfrag(tq + ks * 1024, 256), xf[ks], aq);
        aq = MFMA(corr_a(A_CS(2), hi), cb, aq);
        aq *= rstd * QSCALE;
        u32x4 q0 = pack8(aq, 0), q1 = pack8(aq, 1);
        if (hi) { q1.z = 0u; q1.w = 0u; }
        const int qd = QF_OFF + (j * 64 + rt) * 2 * KB;
        __builtin_amdgcn_raw_buffer_store_b128(q0, rs, l16, qd, 0); __builtin_amdgcn_raw_buffer_store_b128(q1, rs, l16, qd + KB, 0);
        STAMP(0, 5); STAMP(0, 6); STAMP_FLUSH(0, 1);
    }
}

constexpr int B_RED = 0;
constexpr int B_QS = 64 * KB;
constexpr int B_HS = 72 * KB;
constexpr int B_ST = 128 * KB;
constexpr int B_FLAG = B_ST + 4 * KB;
constexpr int B_LDS = B_FLAG + 64 + STAMP_BYTES;

DI void poll_flags(char* ws, int line, int word) {
    GAS unsigned* p = (GAS unsigned*)(ws + FL_OFF) + line * 32 + word;
    for (unsigned spins = 0; spins < (1u << 16); ++spins) {
        if (__all(__hip_atomic_load(p, RLX_AGENT) != 0u)) break;
        __builtin_amdgcn_s_sleep(1);
    }
    __builtin_amdgcn_fence(__ATOMIC_ACQUIRE, "wavefront");
}
DI void red_write(LAS char* lds, int w, int lane, const f32x16& a, const f32x16& c) {
    LAS f32x4* red = (LAS f32x4*)(lds + B_RED) + (w * 8) * 64 + lane;
#pragma unroll
    for (int g = 0; g < 4; ++g) {
        red[g * 64] = f32x4{a[4 * g], a[4 * g + 1], a[4 * g + 2], a[4 * g + 3]};
        red[(4 + g) * 64] = f32x4{c[4 * g], c[4 * g + 1], c[4 * g + 2], c[4 * g + 3]};
    }
}
DI f32x4 red_sum(LAS char* lds, int w, int lane) {
    LAS const f32x4* red = (LAS const f32x4*)(lds + B_RED) + w * 64 + lane;
    f32x4 v[8];
#pragma unroll
    for (int i = 0; i < 8; ++i) v[i] = red[i * 8 * 64];
    return ((v[0] + v[1]) + (v[2] + v[3])) + ((v[4] + v[5]) + (v[6] + v[7]));
}

__global__ __launch_bounds__(512, 2) void kB(const unsigned short* __restrict__ x, char* __restrict__ ws, unsigned short* __restrict__ out) {
    extern __shared__ __attribute__((aligned(16))) char smem[];
    LAS char* lds = (LAS char*)smem;
    constexpr int STAMP_LDS = B_FLAG + 64;
    const int tid = threadIdx.x, lane = tid & 63, w = __builtin_amdgcn_readfirstlane(tid >> 6), r = lane & 31, hi = lane >> 5;
    const int b = blockIdx.x, xcd = b & 7, bi = b >> 3;
    const int h = (xcd & 3) * 4 + (bi & 3), rb = (xcd >> 2) * 8 + (bi >> 2);
    const bool post = bi < 28;
    const int gam = 4 * xcd + bi / 7, c7 = bi % 7;
    const int rtA = (gam >> 1) * 4 + (gam & 1);
    const __amdgpu_buffer_rsrc_t rs = __builtin_amdgcn_make_buffer_rsrc((void*)ws, 0, WS_BYTES, 0x00020000);
    const __amdgpu_buffer_rsrc_t rw = __builtin_amdgcn_make_buffer_rsrc((void*)ws, 0, post ? WS_BYTES : 0, 0x00020000);
    const int l16 = lane * 16;
    STAMP(0, 0);
    u32x4 R[32];
    const int kvo = KV_OFF + (h * 64 + w * 8) * 4 * KB;
    {
        const int qo = QF_OFF + (h * 64 + rb * 4 + (w & 3)) * 2 * KB;
        u32x4 q0 = ldf(rs, l16, qo), q1 = ldf(rs, l16, qo + KB);
#pragma unroll
        for (int i = 0; i < 32; ++i) { R[i] = ldf(rs, l16, kvo + i * KB); if ((i & 3) == 3) SB(); }
        volatile LAS unsigned* flag = (volatile LAS unsigned*)(lds + B_FLAG);
        if (tid == 0) { flag[0] = 0u; flag[1] = 0u; }
        if (w < 4) {
            f32x16 sd = {};
            sd = MFMA(R[0], q0, sd); sd = MFMA(R[1], q1, sd);
            const float m = swap_max(max16(sd));
            if (hi) { const u32x2 p = split3(m); q1.z = p.x; q1.w = p.y; }
            *(LAS u32x4*)(lds + B_QS + w * 2048 + l16) = q0; *(LAS u32x4*)(lds + B_QS + w * 2048 + 1024 + l16) = q1;
        }
    }
    u32x2 xres[2];
    {
        const unsigned short* xp = x + (size_t)((post ? rtA : 0) * 32 + r) * DM + c7 * 64 + (w >> 2) * 32 + (w & 3) * 8 + 4 * hi;
        xres[0] = *(const u32x2*)xp; xres[1] = *(const u32x2*)(xp + 64 * DM);
    }
    BAR();
    STAMP(0, 1);
    const int woo = WO_OFF + (2 * c7 * 32 + 4 * w) * KB, w1o = W1_OFF + (8 * c7 + w) * 28 * KB, w2o = W2_OFF + (2 * c7 * 112 + 14 * w) * KB;
#define NEXTW(i) ((i) < 4 ? woo + (i) * KB : (i) < 8 ? woo + (28 + (i)) * KB : w1o + ((i) - 8) * KB)
#define EXP4(S, i) { S[i] = __builtin_amdgcn_exp2f(S[i]); S[i + 1] = __builtin_amdgcn_exp2f(S[i + 1]); S[i + 2] = __builtin_amdgcn_exp2f(S[i + 2]); S[i + 3] = __builtin_amdgcn_exp2f(S[i + 3]); }
#define PIN(v) asm volatile("" : "+v"(v))
#define STEP(PASS, C, N, OC, OP, QN0, QN1, t, tn, tp, FIRST, LAST, KFREE, VFREE0, VFREE1) { \
        if (!(LAST)) N = MFMA(R[4 * (tn)], QN0, zero16); \
        EXP4(C, 0) p0.x = pk2(C[0], C[1]); p0.y = pk2(C[2], C[3]); PIN(p0); SB(); \
        if (!(FIRST)) OP = MFMA(R[4 * (tp) + 3], p1, OP); \
        if (PASS && VFREE1) R[4 * (tp) + 3] = ldf(rw, l16, NEXTW(4 * (tp) + 3)); \
        EXP4(C, 4) p0.z = pk2(C[4], C[5]); p0.w = pk2(C[6], C[7]); PIN(p0); SB(); \
        if (!(LAST)) N = MFMA(R[4 * (tn) + 1], QN1, N); \
        if (PASS && KFREE) { R[4 * (tn)] = ldf(rw, l16, NEXTW(4 * (tn))); R[4 * (tn) + 1] = ldf(rw, l16, NEXTW(4 * (tn) + 1)); } \
        EXP4(C, 8) p1.x = pk2(C[8], C[9]); p1.y = pk2(C[10], C[11]); PIN(p1); SB(); \
        OC = MFMA(R[4 * (t) + 2], p0, OC); \
        if (PASS && VFREE0) R[4 * (t) + 2] = ldf(rw, l16, NEXTW(4 * (t) + 2)); \
        EXP4(C, 12) p1.z = pk2(C[12], C[13]); p1.w = pk2(C[14], C[15]); PIN(p1); SB(); }
#define TILE(PASS, t) \
        STEP(PASS, sa, sb, oa, ob, qb0, qb1, t, t, (t) - 1 < 0 ? 0 : (t) - 1, (t) == 0, false, true, false, (t) > 0) \
        STEP(PASS, sb, sa, ob, oa, qa0, qa1, t, (t) + 1 > 7 ? 7 : (t) + 1, t, false, (t) == 7, false, true, false)
#define COMBINE(PASS) { \
        const f32x4 o = red_sum(lds, w, lane); \
        LAS const float* lp = (LAS const float*)(lds + B_RED) + (((w >> 2) * 4 + 3) * 64 + 32 + r) * 4; \
        float l = 0.f; _Pragma("unroll") for (int i = 0; i < 8; ++i) l += lp[i * 8 * 64 * 4]; \
        const float il = __builtin_amdgcn_rcpf(l); \
        const int qt = rb * 4 + (PASS) * 2 + (w >> 2); \
        __builtin_amdgcn_raw_buffer_store_b64(u32x2{pk2(o[0] * il, o[1] * il), pk2(o[2] * il, o[3] * il)}, rs, l16 + (w & 1) * 8, OF_OFF + ((qt * 16 + h) * 2 + ((w >> 1) & 1)) * KB, SC1); }
#define FLAG_O(PASS) { if (tid < 2) __hip_atomic_store((GAS unsigned*)(ws + FL_OFF) + (0 * 64 + rb * 4 + (PASS) * 2 + tid) * 32 + h, 1u, RLX_AGENT); }
#define PASS_BEGIN(PASS) \
        u32x4 qa0 = *(LAS const u32x4*)(lds + B_QS + (PASS) * 4096 + l16), qa1 = *(LAS const u32x4*)(lds + B_QS + (PASS) * 4096 + 1024 + l16); \
        u32x4 qb0 = *(LAS const u32x4*)(lds + B_QS + (PASS) * 4096 + 2048 + l16), qb1 = *(LAS const u32x4*)(lds + B_QS + (PASS) * 4096 + 3072 + l16); \
        const f32x16 zero16 = {}; f32x16 oa = {}, ob = {}, sa, sb; u32x4 p0, p1; \
        sa = MFMA(R[0], qa0, zero16); sa = MFMA(R[1], qa1, sa); SB();
#define PASS_END(PASS) \
        ob = MFMA(R[31], p1, ob); \
        if (PASS) R[31] = ldf(rw, l16, NEXTW(31)); \
        { volatile LAS unsigned* flag = (volatile LAS unsigned*)(lds + B_FLAG); \
          const bool bad = hi && !(oa[12] < 1.0e18f && ob[12] < 1.0e18f); \
          if (__any(bad) && lane == 0) flag[PASS] = 1u; \
          red_write(lds, w, lane, oa, ob); \
          BAR(); \
          if (__builtin_expect(flag[PASS] != 0u, 0)) { \
              float ma = -INFINITY, mb = -INFINITY; \
              _Pragma("unroll 1") for (int t = 0; t < 8; ++t) { \
                  const u32x4 k0 = ldf(rs, l16, kvo + t * 4 * KB), k1 = ldf(rs, l16, kvo + t * 4 * KB + KB); \
                  sa = MFMA(k0, qa0, zero16); sa = MFMA(k1, qa1, sa); sb = MFMA(k0, qb0, zero16); sb = MFMA(k1, qb1, sb); \
                  ma = fmaxf(ma, max16(sa)); mb = fmaxf(mb, max16(sb)); } \
              ma = swap_max(ma); mb = swap_max(mb); \
              LAS float* mx = (LAS float*)(lds + B_ST); \
              mx[w * 64 + lane] = hi ? mb : ma; \
              BAR(); \
              _Pragma("unroll") for (int i = 0; i < 8; ++i) { ma = fmaxf(ma, mx[i * 64 + r]); mb = fmaxf(mb, mx[i * 64 + 32 + r]); } \
              if (hi) { const u32x2 pa = split3(ma - (bflo(qa1.z) + bfhi(qa1.z) + bflo(qa1.w))), pb = split3(mb - (bflo(qb1.z) + bfhi(qb1.z) + bflo(qb1.w))); \
                  qa1.z = pa.x; qa1.w = pa.y; qb1.z = pb.x; qb1.w = pb.y; } \
              oa = zero16; ob = zero16; \
              _Pragma("unroll 1") for (int t = 0; t < 8; ++t) { \
                  const u32x4 k0 = ldf(rs, l16, kvo + t * 4 * KB), k1 = ldf(rs, l16, kvo + t * 4 * KB + KB), v0 = ldf(rs, l16, kvo + t * 4 * KB + 2 * KB), v1 = ldf(rs, l16, kvo + t * 4 * KB + 3 * KB); \
                  sa = MFMA(k0, qa0, zero16); sa = MFMA(k1, qa1, sa); sb = MFMA(k0, qb0, zero16); sb = MFMA(k1, qb1, sb); \
                  EXP4(sa, 0) EXP4(sa, 4) EXP4(sa, 8) EXP4(sa, 12) EXP4(sb, 0) EXP4(sb, 4) EXP4(sb, 8) EXP4(sb, 12) \
                  oa = MFMA(v0, pack8(sa, 0), oa); oa = MFMA(v1, pack8(sa, 1), oa); ob = MFMA(v0, pack8(sb, 0), ob); ob = MFMA(v1, pack8(sb, 1), ob); } \
              red_write(lds, w, lane, oa, ob); \
              BAR(); \
          } }
    {
        PASS_BEGIN(0)
        TILE(0, 0) TILE(0, 1) TILE(0, 2) TILE(0, 3) TILE(0, 4) TILE(0, 5) TILE(0, 6) TILE(0, 7)
        STAMP(0, 2);
        PASS_END(0)
        COMBINE(0)
        STAMP(0, 3);
    }
    {
        PASS_BEGIN(1)
        TILE(1, 0) TILE(1, 1)
        VMBAR(7);
        FLAG_O(0)
        TILE(1, 2) TILE(1, 3) TILE(1, 4) TILE(1, 5) TILE(1, 6) TILE(1, 7)
        STAMP(0, 4);
        PASS_END(1)
        COMBINE(1)
    }
    u32x4 E[4];
#pragma unroll
    for (int i = 0; i < 4; ++i) E[i] = ldf(rw, l16, w1o + (24 + i) * KB);
    VMBAR(4);
    FLAG_O(1)
    STAMP(0, 5);
    if (!post) { STAMP_FLUSH(1, 1); return; }
    u32x2 hres0 = {0u, 0u}, hres1 = {0u, 0u};
#pragma unroll 1
    for (int ab = 0; ab < 2; ++ab) {
        const int rt = rtA + 2 * ab;
        poll_flags(ws, 0 * 64 + rt, 2 * w + (lane & 1));
        STAMP(1, 0 + 4 * ab);
        u32x4 of[4];
#pragma unroll
        for (int i = 0; i < 4; ++i) of[i] = ldf1(rs, l16, OF_OFF + (rt * 32 + 4 * w + i) * KB);
        f32x16 a0 = {}, a1 = {};
#pragma unroll
        for (int i = 0; i < 4; ++i) { a0 = MFMA(R[i], of[i], a0); a1 = MFMA(R[4 + i], of[i], a1); }
        STAMP(1, 1 + 4 * ab);
        red_write(lds, w, lane, a0, a1);
        BAR();
        const f32x4 v = red_sum(lds, w, lane);
        const u32x2 xr = ab ? xres[1] : xres[0];
        const u32x2 hv = {pk2(v[0] + bflo(xr.x), v[1] + bfhi(xr.x)), pk2(v[2] + bflo(xr.y), v[3] + bfhi(xr.y))};
        if (ab) hres1 = hv; else hres0 = hv;
        __builtin_amdgcn_raw_buffer_store_b64(hv, rs, l16 + (w & 1) * 8, HF_OFF + (rt * 28 + c7 * 4 + (w >> 1)) * KB, SC1);
        STAMP(1, 2 + 4 * ab);
        DRAIN_BAR();
        if (tid == 0) __hip_atomic_store((GAS unsigned*)(ws + FL_OFF) + (1 * 64 + rt) * 32 + c7, 1u, RLX_AGENT);
        STAMP(1, 3 + 4 * ab);
    }
    float cs1 = 0.f;
#pragma unroll
    for (int i = 0; i < 24; ++i) cs1 = sum8(R[8 + i], cs1);
#pragma unroll
    for (int i = 0; i < 4; ++i) cs1 = sum8(E[i], cs1);
    const u32x4 ca = corr_a(swap_add(cs1), hi);
#pragma unroll 1
    for (int ab = 0; ab < 2; ++ab) {
        const int rt = rtA + 2 * ab;
        LAS char* hs = lds + B_HS + ab * 28 * KB + l16;
        LAS f32x2* st = (LAS f32x2*)(lds + B_ST) + ab * 256;
        if (w < 7) {
            poll_flags(ws, 1 * 64 + rt, w);
            STAMP(2, 0 + 4 * ab);
            u32x4 hf[4];
#pragma unroll
            for (int i = 0; i < 4; ++i) hf[i] = ldf1(rs, l16, HF_OFF + (rt * 28 + 4 * w + i) * KB);
            float s1 = 0.f, s2 = 0.f;
#pragma unroll
            for (int i = 0; i < 4; ++i) { *(LAS u32x4*)(hs + (4 * w + i) * KB) = hf[i]; s1 = sum8(hf[i], s1); s2 = sq8(hf[i], s2); }
            s1 = swap_add(s1); s2 = swap_add(s2);
            if (!hi) st[w * 32 + r] = f32x2{s1, s2};
        }
        BAR();
        STAMP(2, 1 + 4 * ab);
        f32x16 acc = {};
#pragma unroll
        for (int i = 0; i < 24; ++i) acc = MFMA(R[8 + i], *(LAS const u32x4*)(hs + i * KB), acc);
#pragma unroll
        for (int i = 0; i < 4; ++i) acc = MFMA(E[i], *(LAS const u32x4*)(hs + (24 + i) * KB), acc);
        f32x2 sv = st[r];
#pragma unroll
        for (int i = 1; i < 7; ++i) sv += st[i * 32 + r];
        const float mu = sv.x * (1.f / DM), var = fmaxf(sv.y * (1.f / DM) - mu * mu, 0.f), rstd = __builtin_amdgcn_rsqf(var + EPS);
        acc = MFMA(ca, corr_b(mu), acc);
        STAMP(2, 2 + 4 * ab);
        const float nr = -LOG2E * rstd;
#pragma unroll
        for (int i = 0; i < 16; ++i) acc[i] = acc[i] * rstd * __builtin_amdgcn_rcpf(1.f + __builtin_amdgcn_exp2f(acc[i] * nr));
        const int ud = UF_OFF + (rt * 112 + (8 * c7 + w) * 2) * KB;
        __builtin_amdgcn_raw_buffer_store_b128(pack8(acc, 0), rs, l16, ud, SC1); __builtin_amdgcn_raw_buffer_store_b128(pack8(acc, 1), rs, l16, ud + KB, SC1);
        DRAIN_BAR();
        if (tid == 0) __hip_atomic_store((GAS unsigned*)(ws + FL_OFF) + (2 * 64 + rt) * 32 + c7, 1u, RLX_AGENT);
        STAMP(2, 3 + 4 * ab);
    }
#pragma unroll
    for (int i = 0; i < 14; ++i) { R[i] = ldf(rs, l16, w2o + i * KB); R[14 + i] = ldf(rs, l16, w2o + (112 + i) * KB); }
    SB();
#pragma unroll 1
    for (int ab = 0; ab < 2; ++ab) {
        const int rt = rtA + 2 * ab;
        poll_flags(ws, 2 * 64 + rt, (7 * w + 6 * (lane & 1)) >> 3);
        STAMP(3, 0 + 4 * ab);
        u32x4 uf[14];
#pragma unroll
        for (int i = 0; i < 14; ++i) uf[i] = ldf1(rs, l16, UF_OFF + (rt * 112 + 14 * w + i) * KB);
        SB();
        f32x16 a0 = {}, a1 = {};
#pragma unroll
        for (int i = 0; i < 14; ++i) { a0 = MFMA(R[i], uf[i], a0); a1 = MFMA(R[14 + i], uf[i], a1); }
        STAMP(3, 1 + 4 * ab);
        red_write(lds, w, lane, a0, a1);
        BAR();
        const f32x4 v = red_sum(lds, w, lane);
        const u32x2 hr = ab ? hres1 : hres0;
        *(u32x2*)(out + (size_t)(rt * 32 + r) * DM + c7 * 64 + (w >> 2) * 32 + (w & 3) * 8 + 4 * hi) =
            u32x2{pk2(v[0] + bflo(hr.x), v[1] + bfhi(hr.x)), pk2(v[2] + bflo(hr.y), v[3] + bfhi(hr.y))};
        STAMP(3, 2 + 4 * ab);
        BAR();
        STAMP(3, 3 + 4 * ab);
    }
    STAMP_FLUSH(1, 4);
}

extern "C" void kernel_launch(void* const* d_in, const int* in_sizes, int n_in, void* d_out, int out_size, void* d_ws, size_t ws_size, hipStream_t stream) {
    const unsigned short* x = (const unsigned short*)d_in[0];
    const unsigned short* wqkv = (const unsigned short*)d_in[1];
    const unsigned short* wo = (const unsigned short*)d_in[2];
    const unsigned short* w1 = (const unsigned short*)d_in[4];
    const unsigned short* w2 = (const unsigned short*)d_in[5];
    char* ws = (char*)d_ws;
    (void)hipFuncSetAttribute((const void*)kA, hipFuncAttributeMaxDynamicSharedMemorySize, A_LDS);
    (void)hipFuncSetAttribute((const void*)kB, hipFuncAttributeMaxDynamicSharedMemorySize, B_LDS);
    kA<<<256, 512, A_LDS, stream>>>(x, wqkv, wo, w1, w2, ws);
    kB<<<256, 512, B_LDS, stream>>>(x, ws, (unsigned short*)d_out);
}
```

```cpp
#include <hip/hip_runtime.h>
#include <hip/hip_bf16.h>
#include <stdint.h>

typedef short bf16x8 __attribute__((ext_vector_type(8)));
typedef float f32x16 __attribute__((ext_vector_type(16)));
typedef float f32x4 __attribute__((ext_vector_type(4)));
typedef float f32x2 __attribute__((ext_vector_type(2)));
typedef unsigned u32x4 __attribute__((ext_vector_type(4)));
typedef unsigned u32x2 __attribute__((ext_vector_type(2)));
typedef __bf16 bf16x2_t __attribute__((ext_vector_type(2)));
typedef short v4i16_t __attribute__((ext_vector_type(4)));
#define LAS __attribute__((address_space(3)))
#define GAS __attribute__((address_space(1)))
#define DI __device__ __forceinline__
#define MFMA(a, b, c) __builtin_amdgcn_mfma_f32_32x32x16_bf16(__builtin_bit_cast(bf16x8, (a)), __builtin_bit_cast(bf16x8, (b)), (c), 0, 0, 0)
#define BAR() asm volatile("s_waitcnt lgkmcnt(0)\n\ts_barrier" ::: "memory")
#define VMBAR(N) asm volatile("s_waitcnt vmcnt(" #N ") lgkmcnt(0)\n\ts_barrier" ::: "memory")
#define VMWAIT(N) asm volatile("s_waitcnt vmcnt(" #N ")" ::: "memory")
#define SB() __builtin_amdgcn_sched_barrier(0)
#define PIN(v) asm volatile("" : "+v"(v))
#define RLX_AGENT __ATOMIC_RELAXED, __HIP_MEMORY_SCOPE_AGENT

constexpr int SEQ = 2048, DM = 448, NH = 16, DH = 28, FF = 1792;
constexpr float EPS = 1e-5f;
constexpr float LOG2E = 1.4426950408889634f;
constexpr float QSCALE = 0.18898223650461362f * LOG2E;
constexpr unsigned ONES = 0x3f803f80u;
constexpr int KB = 1024;
constexpr int FW_GEN = 0, FW_KV = 64, FW_W = FW_KV + 16 * 32, FW_RT = FW_W + 256;
constexpr int KV_OFF = 32 * KB;
constexpr int OF_OFF = KV_OFF + 16 * 64 * 4 * KB;
constexpr int WO_OFF = OF_OFF + 64 * 32 * KB;
constexpr int HF_OFF = WO_OFF + 14 * 32 * KB;
constexpr int W1_OFF = HF_OFF + 64 * 28 * KB;
constexpr int UF_OFF = W1_OFF + 56 * 28 * KB;
constexpr int W2_OFF = UF_OFF + 64 * 112 * KB;
constexpr int WS_BYTES = W2_OFF + 14 * 112 * KB;
constexpr int SC1 = 16;
static_assert((FW_RT + 3 * 64 * 32) * 4 <= KV_OFF, "flag block");

DI unsigned pk2(float lo, float hi) { f32x2 v = {lo, hi}; return __builtin_bit_cast(unsigned, __builtin_convertvector(v, bf16x2_t)); }
DI float dot2(unsigned a, unsigned b, float c) { return __builtin_amdgcn_fdot2_f32_bf16(__builtin_bit_cast(bf16x2_t, a), __builtin_bit_cast(bf16x2_t, b), c, false); }
DI float sum8(const u32x4 f, float c) { return dot2(f.w, ONES, dot2(f.z, ONES, dot2(f.y, ONES, dot2(f.x, ONES, c)))); }
DI float sq8(const u32x4 f, float c) { return dot2(f.w, f.w, dot2(f.z, f.z, dot2(f.y, f.y, dot2(f.x, f.x, c)))); }
DI float bflo(unsigned u) { return __uint_as_float(u << 16); }
DI float bfhi(unsigned u) { return __uint_as_float(u & 0xffff0000u); }
DI float swap_add(float v) { auto r = __builtin_amdgcn_permlane32_swap(__float_as_uint(v), __float_as_uint(v), false, false); return __uint_as_float(r[0]) + __uint_as_float(r[1]); }
DI float swap_max(float v) { auto r = __builtin_amdgcn_permlane32_swap(__float_as_uint(v), __float_as_uint(v), false, false); return fmaxf(__uint_as_float(r[0]), __uint_as_float(r[1])); }
DI void glds16(const void* sbase, unsigned voff, unsigned lds_base) {
    asm volatile("s_mov_b32 m0, %2\n\ts_nop 0\n\tglobal_load_lds_dwordx4 %0, %1" :: "v"(voff), "s"(sbase), "s"(lds_base) : "memory");
}
DI void glds16c(const void* sbase, unsigned voff, unsigned lds_base) {
    asm volatile("s_mov_b32 m0, %2\n\ts_nop 0\n\tglobal_load_lds_dwordx4 %0, %1 sc1" :: "v"(voff), "s"(sbase), "s"(lds_base) : "memory");
}
DI u32x2 trd(LAS const char* p) { return __builtin_bit_cast(u32x2, __builtin_amdgcn_ds_read_tr16_b64_v4i16((LAS v4i16_t*)p)); }
DI u32x4 trfrag(LAS const char* p, int second) { u32x2 a = trd(p), b = trd(p + second); return u32x4{a.x, a.y, b.x, b.y}; }
DI u32x4 pack8(const f32x16& a, int s) {
    return u32x4{pk2(a[8 * s], a[8 * s + 1]), pk2(a[8 * s + 2], a[8 * s + 3]), pk2(a[8 * s + 4], a[8 * s + 5]), pk2(a[8 * s + 6], a[8 * s + 7])};
}
DI float max16(const f32x16& p) {
    float a = fmaxf(fmaxf(p[0], p[1]), p[2]), b = fmaxf(fmaxf(p[3], p[4]), p[5]);
    a = fmaxf(fmaxf(a, p[6]), p[7]); b = fmaxf(fmaxf(b, p[8]), p[9]);
    a = fmaxf(fmaxf(a, p[10]), p[11]); b = fmaxf(fmaxf(b, p[12]), p[13]);
    a = fmaxf(fmaxf(a, p[14]), p[15]);
    return fmaxf(a, b);
}
DI u32x2 split3(float m) {
    const float v = -m;
    const unsigned u1 = __float_as_uint(v) & 0xffff0000u; const float r1 = v - __uint_as_float(u1);
    const unsigned u2 = __float_as_uint(r1) & 0xffff0000u; const float r2 = r1 - __uint_as_float(u2);
    const unsigned u3 = __float_as_uint(r2) & 0xffff0000u;
    return u32x2{(u1 >> 16) | u2, u3 >> 16};
}
DI u32x4 corr_a(float cs, int hi) {
    const unsigned h = pk2(cs, cs); const float lo = cs - bflo(h); const unsigned l = pk2(lo, lo);
    return hi ? u32x4{0u, 0u, 0u, 0u} : u32x4{h, l, 0u, 0u};
}
DI u32x4 corr_b(float mu) {
    const unsigned h = pk2(-mu, 0.f); const unsigned d = pk2(-mu, -mu - bflo(h));
    return u32x4{d, d, 0u, 0u};
}
DI u32x4 ldf(__amdgpu_buffer_rsrc_t rs, int l16, int soff) { return __builtin_amdgcn_raw_buffer_load_b128(rs, l16, soff, SC1); }
DI void stf(u32x4 v, __amdgpu_buffer_rsrc_t rs, int l16, int soff) { __builtin_amdgcn_raw_buffer_store_b128(v, rs, l16, soff, SC1); }
DI unsigned flag_ld(char* ws, int word) { return __hip_atomic_load((GAS unsigned*)ws + word, RLX_AGENT); }
DI void flag_st(char* ws, int word, unsigned v) { __hip_atomic_store((GAS unsigned*)ws + word, v, RLX_AGENT); }
DI void poll_flags(char* ws, int word, unsigned tag, unsigned first) {
    unsigned v = first;
    for (unsigned spins = 0; !__all(v == tag) && spins < (1u << 17); ++spins) v = flag_ld(ws, word);
    __builtin_amdgcn_fence(__ATOMIC_ACQUIRE, "wavefront");
}

#define STAMP(kid, i)
#define STAMP_FLUSH()
constexpr int STAMP_BYTES = 0;

constexpr int A_XW = 16 * 1040, A_SLOT = A_XW + 8 * KB, A_WQ = A_SLOT, A_PT = A_WQ + 28 * KB + 64, A_CS = 146 * KB;
static_assert(6 * A_SLOT <= A_CS && A_PT + 16 * KB <= 3 * A_SLOT, "phase 1 LDS");
constexpr int B_RED = 0;
constexpr int B_HS = 64 * KB;
constexpr int B_QS = 76 * KB;
constexpr int B_ST = 92 * KB;
constexpr int B_W1 = 96 * KB;
constexpr int B_FLAG = 152 * KB;
constexpr int STAMP_LDS = B_FLAG + 64;
constexpr int LDS_BYTES = STAMP_LDS + STAMP_BYTES;
static_assert(LDS_BYTES <= 160 * KB, "LDS");

DI void red_write(LAS char* lds, int w, int lane, const f32x16& a, const f32x16& c) {
    LAS f32x4* red = (LAS f32x4*)(lds + B_RED) + (w * 8) * 64 + lane;
#pragma unroll
    for (int g = 0; g < 4; ++g) {
        red[g * 64] = f32x4{a[4 * g], a[4 * g + 1], a[4 * g + 2], a[4 * g + 3]};
        red[(4 + g) * 64] = f32x4{c[4 * g], c[4 * g + 1], c[4 * g + 2], c[4 * g + 3]};
    }
}
DI f32x4 red_sum(LAS char* lds, int w, int lane) {
    LAS const f32x4* red = (LAS const f32x4*)(lds + B_RED) + w * 64 + lane;
    f32x4 v[8];
#pragma unroll
    for (int i = 0; i < 8; ++i) v[i] = red[i * 8 * 64];
    SB();
    return ((v[0] + v[1]) + (v[2] + v[3])) + ((v[4] + v[5]) + (v[6] + v[7]));
}

__global__ __launch_bounds__(512, 2) void fused_block(const unsigned short* __restrict__ x, const unsigned short* __restrict__ wqkv, const unsigned short* __restrict__ wo,
                                                      const unsigned short* __restrict__ w1, const unsigned short* __restrict__ w2, char* __restrict__ ws, unsigned short* __restrict__ out) {
    extern __shared__ __attribute__((aligned(16))) char smem[];
    const int tid = threadIdx.x, lane = tid & 63, w = __builtin_amdgcn_readfirstlane(tid >> 6), r = lane & 31, hi = lane >> 5;
    const int b = blockIdx.x, xcd = b & 7, bi = b >> 3;
    unsigned sink;
    {
        unsigned long long pc; unsigned len;
        asm volatile(".Lfb_begin:\n\ts_getpc_b64 %0\n\ts_mov_b32 %1, .Lfb_end-.Lfb_begin" : "=s"(pc), "=s"(len));
        const unsigned off = (unsigned)(((lane & 1) * 8 + w) * 32 + bi) * 128u;
        asm volatile("global_load_dword %0, %1, %2" : "=v"(sink) : "v"(off < len && len < 65536u ? off : 0u), "s"(pc) : "memory");
    }
    asm volatile("" :: "s"(x), "s"(wqkv), "s"(ws));
    LAS char* lds = (LAS char*)smem;
    const unsigned lds0 = (unsigned)(uintptr_t)smem;
    const int h = (xcd & 3) * 4 + (bi & 3), rb = (xcd >> 2) * 8 + (bi >> 2);
    const int rot = (2 * (bi & 3) + (bi >> 2)) % 7;
    const int l16 = lane * 16;
    const int trq = (lane & 15) >> 2, trc = ((lane >> 4) & 1) * 32 + (lane & 3) * 8;
    const __amdgpu_buffer_rsrc_t rs = __builtin_amdgcn_make_buffer_rsrc((void*)ws, 0, WS_BYTES, 0x00020000);
    LAS float* csl = (LAS float*)(lds + A_CS);
    volatile LAS unsigned* lflag = (volatile LAS unsigned*)(lds + B_FLAG);
    STAMP(0, 0);
    unsigned tag;
    u32x4 q0 = {}, q1 = {};
    if (__builtin_expect(w >= 4, 1)) {
        const int L = w - 4;
        const char* xb = (const char*)x + (size_t)(rb * 128 + 64 * (L >> 1) + 4 * (L & 1)) * 896;
        const unsigned xo0 = (lane >> 3) * (8 * 896) + (lane & 7) * 16, xo1 = xo0 + 896, xo2 = xo0 + 2 * 896, xo3 = xo0 + 3 * 896;
        const char* wkv = (const char*)wqkv + (size_t)(16 * L) * 2688 + 896 + 112 * h;
        const unsigned woff = (lane >> 3) * 2688 + (lane & 7) * 16, woff2 = woff + 8 * 2688;
#pragma unroll 1
        for (int s = 0; s < 7; ++s) {
            int kb = s + rot; kb = kb >= 7 ? kb - 7 : kb;
            const unsigned d_ = lds0 + (s == 6 ? 0 : s) * A_SLOT;
            if (s == 6) { while (lflag[4] < 4u) __builtin_amdgcn_s_sleep(1); }
            glds16(xb + kb * 128, xo0, d_ + L * 4160); glds16(xb + kb * 128, xo1, d_ + L * 4160 + 1040);
            glds16(xb + kb * 128, xo2, d_ + L * 4160 + 2080); glds16(xb + kb * 128, xo3, d_ + L * 4160 + 3120);
            if ((lane & 7) != 7) { glds16(wkv + (size_t)kb * (64 * 2688), woff, d_ + A_XW + L * 2048); glds16(wkv + (size_t)kb * (64 * 2688), woff2, d_ + A_XW + L * 2048 + 1024); }
            if (s == 0) BAR();
            else { VMWAIT(6); if (lane == 0) __hip_atomic_fetch_add((LAS unsigned*)&lflag[8 + s - 1], 1u, __ATOMIC_RELAXED, __HIP_MEMORY_SCOPE_WORKGROUP); }
        }
        VMWAIT(0);
        if (lane == 0) __hip_atomic_fetch_add((LAS unsigned*)&lflag[8 + 6], 1u, __ATOMIC_RELAXED, __HIP_MEMORY_SCOPE_WORKGROUP);
        PIN(sink);
        while (lflag[5] < 4u) __builtin_amdgcn_s_sleep(1);
        {
            const char* wq = (const char*)wqkv + (size_t)(16 * L) * 2688 + ((56 * h) & ~15);
            const unsigned qoff = (lane >> 2) * 2688 + (lane & 3) * 16;
#pragma unroll 1
            for (int i = 0; i < 7; ++i) { int kb = i + rot; kb = kb >= 7 ? kb - 7 : kb; glds16(wq + (size_t)kb * (64 * 2688), qoff, lds0 + A_WQ + (4 * i + L) * 1024); }
        }
        const char* psrc; int ppitch, pdst, pstep, k0;
        if (b < 112) { const int cp = b >> 2; k0 = (b & 3) * 7; psrc = (const char*)w1 + cp * 128; ppitch = FF * 2; pdst = W1_OFF + (cp * 2 * 28) * KB; pstep = 28 * KB; }
        else if (b < 224) { const int q = b - 112, cp = q >> 4; k0 = (q & 15) * 7; psrc = (const char*)w2 + cp * 128; ppitch = DM * 2; pdst = W2_OFF + (cp * 2 * 112) * KB; pstep = 112 * KB; }
        else { k0 = (b - 224) * 7; psrc = (const char*)wo; ppitch = DM * 2; pdst = WO_OFF; pstep = 32 * KB; }
        int pks[2], pd[2];
#pragma unroll
        for (int i = 0; i < 2; ++i) {
            const int kk = k0 + ((L + 4 * i < 7) ? L + 4 * i : L);
            const int cp = kk >> 5, ks = kk & 31;
            pks[i] = (b >= 224) ? (ks & 1) : 0; pd[i] = (b >= 224) ? pdst + (cp * 64 + ks) * KB : pdst + kk * KB;
#pragma unroll
            for (int u = 0; u < 2; ++u) {
                const int row = 8 * u + (lane >> 3), c = 16 * (ks & 1) + row;
                const int srow = (b >= 224) ? (ks >> 1) * DH + (c < DH ? c : DH - 1) : 16 * kk + row;
                glds16(psrc, (unsigned)(srow * ppitch + ((b >= 224) ? cp * 128 : 0) + (lane & 7) * 16), lds0 + A_PT + ((L * 2 + i) * 2 + u) * 1024);
            }
        }
        VMWAIT(4);
        float cq = 0.f;
        LAS const char* tq = lds + A_WQ + L * 1024 + (8 * hi + trq) * 64 + trc + ((56 * h) & 15);
#pragma unroll
        for (int i = 0; i < 7; ++i) cq = sum8(trfrag(tq + i * 4096, 256), cq);
        csl[(2 * 32 + r) * 8 + 2 * L + hi] = cq;
        STAMP(0, 4);
        BAR();
        tag = __builtin_amdgcn_readfirstlane(lflag[2]);
        LAS const char* tp = lds + A_PT + L * 4096 + (4 * hi + trq) * 128 + trc;
        VMWAIT(0);
#pragma unroll
        for (int i = 0; i < 2; ++i) {
            if (L + 4 * i < 7) {
#pragma unroll
                for (int ct = 0; ct < 2; ++ct) {
                    u32x4 f = trfrag(tp + i * 2048 + ct * 64, 1024);
                    if (pks[i] && hi) { f.z = 0u; f.w = 0u; }
                    stf(f, rs, l16, pd[i] + ct * pstep);
                }
            }
        }
        STAMP(0, 5);
    } else {
        const int rt = rb * 4 + w;
        const unsigned gen = flag_ld(ws, FW_GEN);
        if (tid < 16) lflag[tid] = 0u;
        if ((lane & 7) == 7) {
#pragma unroll
            for (int s = 0; s < 12; ++s) *(LAS u32x4*)(lds + (s >> 1) * A_SLOT + A_XW + w * 2048 + (s & 1) * 1024 + l16) = u32x4{ONES, ONES, ONES, ONES};
        }
        BAR();
        LAS const char* xr = lds + (8 * (w >> 1) + (r & 7)) * 1040 + (4 * (w & 1) + (r >> 3)) * 128 + hi * 16;
        LAS const char* tw = lds + A_XW + (8 * hi + trq) * 128 + trc;
        u32x4 xf[28];
        f32x16 a0 = {}, a1 = {};
        float s2 = 0.f, c0 = 0.f, c1 = 0.f;
        u32x4 wa[2][4], wb[2][4];
#define A_READ(s) { while (lflag[8 + (s)] < 4u) {} \
        asm volatile("" ::: "memory"); \
        _Pragma("unroll") for (int i = 0; i < 4; ++i) { \
            xf[4 * (s) + i] = *(LAS const u32x4*)(xr + ((s) % 6) * A_SLOT + (i ^ w) * 32); \
            wa[(s) & 1][i] = trfrag(tw + ((s) % 6) * A_SLOT + (i ^ w) * 2048, 512); wb[(s) & 1][i] = trfrag(tw + ((s) % 6) * A_SLOT + (i ^ w) * 2048 + 56, 512); } \
        SB(); }
#define A_MM(s, i) { a0 = MFMA(wa[(s) & 1][i], xf[4 * (s) + i], a0); a1 = MFMA(wb[(s) & 1][i], xf[4 * (s) + i], a1); PIN(a0); PIN(a1); SB(); }
#define A_STAGE(s, DONE) { A_MM(s, 0) A_MM(s, 1) \
        if ((DONE) && lane == 0) { asm volatile("s_waitcnt lgkmcnt(0)" ::: "memory"); __hip_atomic_fetch_add((LAS unsigned*)&lflag[DONE], 1u, __ATOMIC_RELAXED, __HIP_MEMORY_SCOPE_WORKGROUP); } \
        if ((s) < 6) A_READ(((s) < 6 ? (s) + 1 : 6)) \
        A_MM(s, 2) A_MM(s, 3) \
        _Pragma("unroll") for (int i = 0; i < 4; ++i) s2 = sq8(xf[4 * (s) + i], s2); \
        c0 = sum8(wa[(s) & 1][0], c0); c1 = sum8(wb[(s) & 1][0], c1); PIN(s2); PIN(c0); PIN(c1); SB(); }
        A_READ(0)
        A_STAGE(0, 4)
        STAMP(0, 1);
        A_STAGE(1, 0) A_STAGE(2, 5) A_STAGE(3, 0)
        STAMP(0, 2);
        A_STAGE(4, 0) A_STAGE(5, 0)
        csl[(0 * 32 + r) * 8 + 2 * w + hi] = c0; csl[(1 * 32 + r) * 8 + 2 * w + hi] = c1;
        asm volatile("s_waitcnt lgkmcnt(0)" ::: "memory");
        if (lane == 0) __hip_atomic_fetch_add((LAS unsigned*)&lflag[6], 1u, __ATOMIC_RELAXED, __HIP_MEMORY_SCOPE_WORKGROUP);
        c0 = 0.f; c1 = 0.f;
        A_STAGE(6, 0)
#pragma unroll
        for (int i = 1; i < 4; ++i) { c0 = sum8(wa[0][i], c0); c1 = sum8(wb[0][i], c1); }
        STAMP(0, 3);
        tag = __builtin_amdgcn_readfirstlane(gen) + 1u;
        PIN(sink);
        if (tid == 0) lflag[2] = tag;
        s2 = swap_add(s2);
        const float mu = swap_add(hi ? a1[12] : 0.f) * (1.f / DM), var = fmaxf(s2 * (1.f / DM) - mu * mu, 0.f), rstd = __builtin_amdgcn_rsqf(var + EPS);
        const u32x4 cb = corr_b(mu);
        const int dst = KV_OFF + (((2 * h) & 15) * 64 + rt) * 4 * KB + (h < 8 ? 0 : 2 * KB);
        while (lflag[6] < 4u) {}
        asm volatile("" ::: "memory");
#define A_CS(t) ({ const f32x4 p0 = *(LAS const f32x4*)(csl + ((t) * 32 + r) * 8), p1 = *(LAS const f32x4*)(csl + ((t) * 32 + r) * 8 + 4); ((p0.x + p0.y) + (p0.z + p0.w)) + ((p1.x + p1.y) + (p1.z + p1.w)); })
        const float cs0 = A_CS(0) + swap_add(c0), cs1 = A_CS(1) + swap_add(c1);
        a0 = MFMA(corr_a(cs0, hi), cb, a0); a1 = MFMA(corr_a(cs1, hi), cb, a1);
        a0 *= rstd; a1 *= rstd;
        u32x4 f00 = pack8(a0, 0), f01 = pack8(a0, 1), f10 = pack8(a1, 0), f11 = pack8(a1, 1);
        const unsigned pz = h < 8 ? ONES : 0x00003f80u, pw = h < 8 ? 0x00003f80u : 0u;
        if (hi) { f01.z = pz; f01.w = pw; f11.z = pz; f11.w = pw; }
        if (h >= 8) {
            LAS char* tb = lds + 4 * A_SLOT + w * 4096;
            LAS char* wr = tb + r * 64 + hi * 8;
            *(LAS u32x2*)(wr) = u32x2{f00.x, f00.y}; *(LAS u32x2*)(wr + 16) = u32x2{f00.z, f00.w}; *(LAS u32x2*)(wr + 32) = u32x2{f01.x, f01.y}; *(LAS u32x2*)(wr + 48) = u32x2{f01.z, f01.w};
            *(LAS u32x2*)(wr + 2048) = u32x2{f10.x, f10.y}; *(LAS u32x2*)(wr + 2064) = u32x2{f10.z, f10.w}; *(LAS u32x2*)(wr + 2080) = u32x2{f11.x, f11.y}; *(LAS u32x2*)(wr + 2096) = u32x2{f11.z, f11.w};
            asm volatile("s_waitcnt lgkmcnt(0)" ::: "memory");
            LAS const char* rd = tb + (4 * hi + trq) * 64 + trc;
            f00 = trfrag(rd, 512); f01 = trfrag(rd + 1024, 512); f10 = trfrag(rd + 2048, 512); f11 = trfrag(rd + 3072, 512);
        }
        stf(f00, rs, l16, dst); stf(f01, rs, l16, dst + KB); stf(f10, rs, l16, dst + 64 * 4 * KB); stf(f11, rs, l16, dst + 64 * 4 * KB + KB);
        STAMP(0, 4);
        VMWAIT(0);
        if (lane == 0 && __hip_atomic_fetch_add((LAS unsigned*)&lflag[7], 1u, __ATOMIC_RELAXED, __HIP_MEMORY_SCOPE_WORKGROUP) == 3u) flag_st(ws, FW_KV + h * 32 + rb, tag);
        BAR();
        LAS const char* tq = lds + A_WQ + (8 * hi + trq) * 64 + trc + ((56 * h) & 15);
        f32x16 aq = {};
        u32x4 wq[8];
#pragma unroll
        for (int i = 0; i < 8; ++i) wq[i] = trfrag(tq + (i >> 2) * 4096 + ((i & 3) ^ w) * 1024, 256);
        SB();
#pragma unroll
        for (int i = 0; i < 28; ++i) {
            aq = MFMA(wq[i & 7], xf[i], aq); SB();
            if (i + 8 < 28) wq[i & 7] = trfrag(tq + ((i + 8) >> 2) * 4096 + ((i & 3) ^ w) * 1024, 256);
            SB();
        }
        aq = MFMA(corr_a(A_CS(2), hi), cb, aq);
        aq *= rstd * QSCALE;
        q0 = pack8(aq, 0); q1 = pack8(aq, 1);
        if (hi) { q1.z = 0u; q1.w = 0u; }
        STAMP(0, 5);
    }
    const bool post = bi < 28;
    const int gam = 4 * xcd + bi / 7, c7 = bi % 7;
    const int rtA = (gam >> 1) * 4 + (gam & 1);
    const __amdgpu_buffer_rsrc_t rw = __builtin_amdgcn_make_buffer_rsrc((void*)ws, 0, post ? WS_BYTES : 0, 0x00020000);
    u32x4 R[32];
    const int kvo = KV_OFF + (h * 64 + w * 8) * 4 * KB;
#define KVA(i) (kvo + ((((i) + 4 * rb) & 31) * KB))
    {
        const int fw = FW_KV + ((h >> 1) + 8 * (lane & 1)) * 32 + 2 * w + ((lane >> 1) & 1);
        poll_flags(ws, fw, tag, tag + 1u);
        STAMP(0, 6);
#pragma unroll
        for (int i = 0; i < 8; ++i) { R[i] = ldf(rs, l16, KVA(i)); SB(); }
        if (w < 4) {
            f32x16 sd = {};
            sd = MFMA(R[0], q0, sd); sd = MFMA(R[1], q1, sd);
            const float m = swap_max(max16(sd));
            if (hi) { const u32x2 p = split3(m); q1.z = p.x; q1.w = p.y; }
            *(LAS u32x4*)(lds + B_QS + w * 2048 + l16) = q0; *(LAS u32x4*)(lds + B_QS + w * 2048 + 1024 + l16) = q1;
        }
    }
    BAR();
    if (tid == 256) flag_st(ws, FW_W + b, tag);
    STAMP(0, 7);
    const int woo = WO_OFF + (2 * c7 * 32 + 4 * w) * KB, w1o = W1_OFF + (8 * c7 + w) * 28 * KB, w2o = W2_OFF + (2 * c7 * 112 + 14 * w) * KB;
#define NEXTW(i) ((i) < 4 ? woo + (i) * KB : (i) < 8 ? woo + (28 + (i)) * KB : w1o + ((i) - 8) * KB)
#define EXP4(S, i) { S[i] = __builtin_amdgcn_exp2f(S[i]); S[i + 1] = __builtin_amdgcn_exp2f(S[i + 1]); S[i + 2] = __builtin_amdgcn_exp2f(S[i + 2]); S[i + 3] = __builtin_amdgcn_exp2f(S[i + 3]); }
#define STEP(PASS, C, N, OC, OP, QN0, QN1, t, tn, tp, FIRST, LAST, KFREE, VFREE0, VFREE1, KVL) { \
        if (!(LAST)) { N = MFMA(R[4 * (tn)], QN0, zero16); SB(); } \
        if (!PASS && (KVL) < 32) R[(KVL) & 31] = ldf(rs, l16, KVA(KVL)); \
        EXP4(C, 0) p0.x = pk2(C[0], C[1]); p0.y = pk2(C[2], C[3]); PIN(p0); SB(); \
        if (!(FIRST)) { OP = MFMA(R[4 * (tp) + 3], p1, OP); SB(); } \
        if (PASS && VFREE1 && (tp) < 6) R[4 * (tp) + 3] = ldf(rw, l16, NEXTW(4 * (tp) + 3)); \
        EXP4(C, 4) p0.z = pk2(C[4], C[5]); p0.w = pk2(C[6], C[7]); PIN(p0); SB(); \
        if (!(LAST)) { N = MFMA(R[4 * (tn) + 1], QN1, N); SB(); } \
        if (!PASS && (KVL) < 32) R[((KVL) + 1) & 31] = ldf(rs, l16, KVA((KVL) + 1)); \
        if (PASS && KFREE && (tn) < 6) { R[4 * (tn)] = ldf(rw, l16, NEXTW(4 * (tn))); R[4 * (tn) + 1] = ldf(rw, l16, NEXTW(4 * (tn) + 1)); } \
        EXP4(C, 8) p1.x = pk2(C[8], C[9]); p1.y = pk2(C[10], C[11]); PIN(p1); SB(); \
        OC = MFMA(R[4 * (t) + 2], p0, OC); SB(); \
        if (PASS && VFREE0 && (t) < 6) R[4 * (t) + 2] = ldf(rw, l16, NEXTW(4 * (t) + 2)); \
        EXP4(C, 12) p1.z = pk2(C[12], C[13]); p1.w = pk2(C[14], C[15]); PIN(p1); SB(); }
#define TILE(PASS, t) \
        STEP(PASS, sa, sb, oa, ob, qb0, qb1, t, t, (t) - 1 < 0 ? 0 : (t) - 1, (t) == 0, false, true, false, (t) > 0, 4 * (t) + 8) \
        STEP(PASS, sb, sa, ob, oa, qa0, qa1, t, (t) + 1 > 7 ? 7 : (t) + 1, t, false, (t) == 7, false, true, false, 4 * (t) + 10)
#define COMBINE(PASS) { \
        LAS const float* lp = (LAS const float*)(lds + B_RED) + (((w >> 2) * 4 + 3) * 64 + 32 + r) * 4; \
        float lv[8]; _Pragma("unroll") for (int i = 0; i < 8; ++i) lv[i] = lp[i * 8 * 64 * 4]; \
        const f32x4 o = red_sum(lds, w, lane); \
        const float il = __builtin_amdgcn_rcpf(((lv[0] + lv[1]) + (lv[2] + lv[3])) + ((lv[4] + lv[5]) + (lv[6] + lv[7]))); \
        const int qt = rb * 4 + (PASS) * 2 + (w >> 2); \
        __builtin_amdgcn_raw_buffer_store_b64(u32x2{pk2(o[0] * il, o[1] * il), pk2(o[2] * il, o[3] * il)}, rs, l16 + (w & 1) * 8, OF_OFF + ((qt * 16 + h) * 2 + ((w >> 1) & 1)) * KB, SC1); }
#define FLAG_O(PASS) { if (tid < 2) flag_st(ws, FW_RT + (0 * 64 + rb * 4 + (PASS) * 2 + tid) * 32 + h, tag); }
#define LOADQ(PASS) \
        qa0 = *(LAS const u32x4*)(lds + B_QS + (PASS) * 4096 + l16); qa1 = *(LAS const u32x4*)(lds + B_QS + (PASS) * 4096 + 1024 + l16); \
        qb0 = *(LAS const u32x4*)(lds + B_QS + (PASS) * 4096 + 2048 + l16); qb1 = *(LAS const u32x4*)(lds + B_QS + (PASS) * 4096 + 3072 + l16);
#define PASS_BEGIN(PASS) LOADQ(PASS) oa = zero16; ob = zero16; sa = MFMA(R[0], qa0, zero16); sa = MFMA(R[1], qa1, sa); SB();
#define PASS_END(PASS) \
        ob = MFMA(R[31], p1, ob); \
        { const bool bad = hi && !(oa[12] < 1.0e18f && ob[12] < 1.0e18f); \
          if (__any(bad) && lane == 0) lflag[PASS] = 1u; } \
        red_write(lds, w, lane, oa, ob);
    u32x2 xres0, xres1;
    unsigned pfo;
    {
        const f32x16 zero16 = {};
        u32x4 qa0, qa1, qb0, qb1, p0, p1; f32x16 oa, ob, sa, sb;
        PASS_BEGIN(0)
        TILE(0, 0) TILE(0, 1) TILE(0, 2) TILE(0, 3) TILE(0, 4) TILE(0, 5)
        const u32x4 wfl = __builtin_amdgcn_raw_buffer_load_b128(rs, l16, FW_W * 4, SC1);
        TILE(0, 6) TILE(0, 7)
        STAMP(1, 0);
        PASS_END(0)
        {
            u32x4 v = wfl;
            for (unsigned spins = 0; !__all(v.x == tag && v.y == tag && v.z == tag && v.w == tag) && spins < (1u << 17); ++spins) {
                v.x = flag_ld(ws, FW_W + 4 * lane); v.y = flag_ld(ws, FW_W + 4 * lane + 1); v.z = flag_ld(ws, FW_W + 4 * lane + 2); v.w = flag_ld(ws, FW_W + 4 * lane + 3);
            }
            __builtin_amdgcn_fence(__ATOMIC_ACQUIRE, "wavefront");
        }
        if (post) {
#pragma unroll 1
            for (int i = 0; i < 7; ++i) glds16c(ws + w1o + (16 + i) * KB, l16, lds0 + B_W1 + (w * 7 + i) * KB);
        }
        PASS_BEGIN(1)
        TILE(1, 0) TILE(1, 1)
        BAR();
        STAMP(1, 1);
        const bool okA = lflag[0] == 0u;
        if (okA) COMBINE(0)
        TILE(1, 2) TILE(1, 3) TILE(1, 4) TILE(1, 5)
        VMBAR(16);
        if (okA) FLAG_O(0)
        STAMP(1, 2);
        {
            const unsigned short* xp = x + (size_t)((post ? rtA : 0) * 32 + r) * DM + c7 * 64 + (w >> 2) * 32 + (w & 3) * 8 + 4 * hi;
            xres0 = *(const u32x2*)xp; xres1 = *(const u32x2*)(xp + 64 * DM);
        }
        TILE(1, 6) TILE(1, 7)
        pfo = flag_ld(ws, FW_RT + (0 * 64 + (post ? rtA : 0)) * 32 + 2 * w + (lane & 1));
        STAMP(1, 3);
        PASS_END(1)
        BAR();
        if (__builtin_expect(lflag[0] != 0u || lflag[1] != 0u, 0)) {
            if (lflag[1] == 0u) COMBINE(1)
#pragma unroll 1
            for (int p = 0; p < 2; ++p) {
                if (lflag[p] == 0u) continue;
                LOADQ(p)
                float ma = -INFINITY, mb = -INFINITY;
#pragma unroll 1
                for (int t = 0; t < 8; ++t) {
                    const u32x4 k0 = ldf(rs, l16, kvo + t * 4 * KB), k1 = ldf(rs, l16, kvo + t * 4 * KB + KB);
                    sa = MFMA(k0, qa0, zero16); sa = MFMA(k1, qa1, sa); sb = MFMA(k0, qb0, zero16); sb = MFMA(k1, qb1, sb);
                    ma = fmaxf(ma, max16(sa)); mb = fmaxf(mb, max16(sb));
                }
                ma = swap_max(ma); mb = swap_max(mb);
                LAS float* mx = (LAS float*)(lds + B_ST);
                mx[w * 64 + lane] = hi ? mb : ma;
                BAR();
#pragma unroll
                for (int i = 0; i < 8; ++i) { ma = fmaxf(ma, mx[i * 64 + r]); mb = fmaxf(mb, mx[i * 64 + 32 + r]); }
                if (hi) {
                    const u32x2 pa = split3(ma - (bflo(qa1.z) + bfhi(qa1.z) + bflo(qa1.w))), pb = split3(mb - (bflo(qb1.z) + bfhi(qb1.z) + bflo(qb1.w)));
                    qa1.z = pa.x; qa1.w = pa.y; qb1.z = pb.x; qb1.w = pb.y;
                }
                oa = zero16; ob = zero16;
#pragma unroll 1
                for (int t = 0; t < 8; ++t) {
                    const u32x4 k0 = ldf(rs, l16, kvo + t * 4 * KB), k1 = ldf(rs, l16, kvo + t * 4 * KB + KB), v0 = ldf(rs, l16, kvo + t * 4 * KB + 2 * KB), v1 = ldf(rs, l16, kvo + t * 4 * KB + 3 * KB);
                    sa = MFMA(k0, qa0, zero16); sa = MFMA(k1, qa1, sa); sb = MFMA(k0, qb0, zero16); sb = MFMA(k1, qb1, sb);
                    EXP4(sa, 0) EXP4(sa, 4) EXP4(sa, 8) EXP4(sa, 12) EXP4(sb, 0) EXP4(sb, 4) EXP4(sb, 8) EXP4(sb, 12)
                    oa = MFMA(v0, pack8(sa, 0), oa); oa = MFMA(v1, pack8(sa, 1), oa); ob = MFMA(v0, pack8(sb, 0), ob); ob = MFMA(v1, pack8(sb, 1), ob);
                }
                red_write(lds, w, lane, oa, ob);
                BAR();
                COMBINE(p)
                VMBAR(0);
                if (p == 0) FLAG_O(0)
            }
        } else COMBINE(1)
    }
    if (!post) { VMBAR(0); FLAG_O(1) STAMP_FLUSH(); return; }
    const int rtB = rtA + 2;
    const int ou = l16 + (w & 1) * 8;
    u32x4 E[5], of[4];
    f32x16 a0, a1;
#define WO_LOAD(rt) { _Pragma("unroll") for (int i = 0; i < 4; ++i) of[i] = ldf(rs, l16, OF_OFF + ((rt) * 32 + 4 * w + i) * KB); SB(); }
#define WO_MFMA() { a0 = f32x16{}; a1 = f32x16{}; _Pragma("unroll") for (int i = 0; i < 4; ++i) { a0 = MFMA(R[i], of[i], a0); a1 = MFMA(R[4 + i], of[i], a1); } }
#define WO_FIN(rt, xr, hr) { const f32x4 v = red_sum(lds, w, lane); \
        hr = u32x2{pk2(v[0] + bflo(xr.x), v[1] + bfhi(xr.x)), pk2(v[2] + bflo(xr.y), v[3] + bfhi(xr.y))}; \
        __builtin_amdgcn_raw_buffer_store_b64(hr, rs, ou, HF_OFF + ((rt) * 28 + c7 * 4 + (w >> 1)) * KB, SC1); }
#define ROT14(k) ((k) + 2 * c7 >= 14 ? (k) + 2 * c7 - 14 : (k) + 2 * c7)
#define W2R(i) ((i) < 8 ? 24 + (i) : (i) - 8)
#define W2_LOAD(lo, hi_) { _Pragma("unroll") for (int i = (lo); i < (hi_); ++i) R[W2R(i)] = ldf(rs, l16, w2o + (ROT14(i >> 1) + (i & 1) * 112) * KB); SB(); }
    u32x2 hres0, hres1;
    poll_flags(ws, FW_RT + (0 * 64 + rtA) * 32 + 2 * w + (lane & 1), tag, pfo);
    WO_LOAD(rtA)
    VMBAR(4);
    FLAG_O(1)
    STAMP(1, 4);
#pragma unroll
    for (int i = 0; i < 5; ++i) E[i] = ldf(rs, l16, w1o + (23 + i) * KB);
    W2_LOAD(0, 4)
    WO_MFMA()
    red_write(lds, w, lane, a0, a1);
    BAR();
    WO_FIN(rtA, xres0, hres0)
    STAMP(1, 5);
    float cs1 = 0.f;
    LAS const char* w1l = lds + B_W1 + w * 7 * KB + l16;
#pragma unroll
    for (int i = 0; i < 16; ++i) cs1 = sum8(R[8 + i], cs1);
#pragma unroll
    for (int i = 0; i < 7; ++i) cs1 = sum8(*(LAS const u32x4*)(w1l + i * KB), cs1);
    PIN(cs1); SB();
    poll_flags(ws, FW_RT + (0 * 64 + rtB) * 32 + 2 * w + (lane & 1), tag, tag + 1u);
    WO_LOAD(rtB)
    BAR();
    if (tid == 0) flag_st(ws, FW_RT + (1 * 64 + rtA) * 32 + c7, tag);
    STAMP(1, 6);
    W2_LOAD(4, 8)
    WO_MFMA()
    red_write(lds, w, lane, a0, a1);
    BAR();
    WO_FIN(rtB, xres1, hres1)
    STAMP(1, 7);
#pragma unroll
    for (int i = 0; i < 5; ++i) cs1 = sum8(E[i], cs1);
    const u32x4 ca = corr_a(swap_add(cs1), hi);
    const int wp = w < 7 ? w : 6;
#define H_LOAD(rt) { _Pragma("unroll") for (int i = 0; i < 4; ++i) of[i] = ldf(rs, l16, HF_OFF + ((rt) * 28 + 4 * wp + i) * KB); SB(); }
#define H_STAGE(hs, ab) { float s1 = 0.f, s2 = 0.f; \
        _Pragma("unroll") for (int i = 0; i < 4; ++i) { *(LAS u32x4*)(lds + (hs) + l16 + (4 * wp + i) * KB) = of[i]; s1 = sum8(of[i], s1); s2 = sq8(of[i], s2); } \
        s1 = swap_add(s1); s2 = swap_add(s2); ((LAS f32x2*)(lds + B_ST))[(ab) * 224 + wp * 32 + r] = f32x2{s1, s2}; }
#define F1_MFMA(hs, HOOK) { u32x4 hb[4], wl[4]; a0 = f32x16{}; \
        _Pragma("unroll") for (int i = 0; i < 4; ++i) hb[i] = *(LAS const u32x4*)(lds + (hs) + l16 + i * KB); \
        SB(); \
        _Pragma("unroll") for (int i = 0; i < 28; ++i) { \
            a0 = MFMA(i < 16 ? R[8 + (i < 16 ? i : 0)] : i < 23 ? wl[i & 3] : E[i < 23 ? 0 : i - 23], hb[i & 3], a0); SB(); \
            if (i + 4 < 28) hb[i & 3] = *(LAS const u32x4*)(lds + (hs) + l16 + (i + 4) * KB); \
            if (i + 4 >= 16 && i + 4 < 23) wl[i & 3] = *(LAS const u32x4*)(w1l + (i + 4 - 16) * KB); \
            HOOK; SB(); } }
#define F1_FIN(rt, ab) { LAS const f32x2* st = (LAS const f32x2*)(lds + B_ST) + (ab) * 224 + r; f32x2 sv = st[0]; \
        _Pragma("unroll") for (int i = 1; i < 7; ++i) sv += st[i * 32]; \
        const float mu = sv.x * (1.f / DM), var = fmaxf(sv.y * (1.f / DM) - mu * mu, 0.f), rstd = __builtin_amdgcn_rsqf(var + EPS); \
        a0 = MFMA(ca, corr_b(mu), a0); \
        const float nr = -LOG2E * rstd; \
        _Pragma("unroll") for (int i = 0; i < 16; ++i) a0[i] = a0[i] * rstd * __builtin_amdgcn_rcpf(1.f + __builtin_amdgcn_exp2f(a0[i] * nr)); \
        const int ud = UF_OFF + ((rt) * 112 + (8 * c7 + w) * 2) * KB; \
        stf(pack8(a0, 0), rs, l16, ud); stf(pack8(a0, 1), rs, l16, ud + KB); }
    poll_flags(ws, FW_RT + (1 * 64 + rtA) * 32 + wp, tag, tag + 1u);
    H_LOAD(rtA)
    H_STAGE(B_HS, 0)
    BAR();
    if (tid == 0) flag_st(ws, FW_RT + (1 * 64 + rtB) * 32 + c7, tag);
    STAMP(2, 0);
    unsigned pf;
    F1_MFMA(B_HS, if (i < 8) R[i] = ldf(rs, l16, w2o + (ROT14((i + 8) >> 1) + (i & 1) * 112) * KB); if (i == 20) pf = flag_ld(ws, FW_RT + (1 * 64 + rtB) * 32 + wp))
    STAMP(2, 1);
    poll_flags(ws, FW_RT + (1 * 64 + rtB) * 32 + wp, tag, pf);
    H_LOAD(rtB)
    STAMP(2, 2);
    F1_FIN(rtA, 0)
    H_STAGE(B_RED, 1)
    STAMP(2, 3);
    VMBAR(0);
    if (tid == 0) flag_st(ws, FW_RT + (2 * 64 + rtA) * 32 + c7, tag);
    STAMP(2, 4);
    F1_MFMA(B_RED, if (i < 12) R[8 + (i < 12 ? i : 0)] = ldf(rs, l16, w2o + (ROT14(((i < 12 ? i : 0) + 16) >> 1) + (i & 1) * 112) * KB))
    STAMP(2, 5);
    const int fu = (7 * w + 6 * (lane & 1)) >> 3;
    pf = flag_ld(ws, FW_RT + (2 * 64 + rtA) * 32 + fu);
    F1_FIN(rtB, 1)
    STAMP(2, 6);
    VMBAR(0);
    if (tid == 0) flag_st(ws, FW_RT + (2 * 64 + rtB) * 32 + c7, tag);
    STAMP(2, 7);
#pragma unroll 1
    for (int ab = 0; ab < 2; ++ab) {
        const int rt = rtA + 2 * ab;
        poll_flags(ws, FW_RT + (2 * 64 + rt) * 32 + fu, tag, pf);
        STAMP(3, 0 + 4 * ab);
        u32x4 uf[14];
#pragma unroll
        for (int i = 0; i < 14; ++i) uf[i] = ldf(rs, l16, UF_OFF + (rt * 112 + 14 * w + ROT14(i)) * KB);
        pf = flag_ld(ws, FW_RT + (2 * 64 + rtB) * 32 + fu);
        SB();
        a0 = f32x16{}; a1 = f32x16{};
#pragma unroll
        for (int i = 0; i < 14; ++i) { a0 = MFMA(R[W2R(2 * i)], uf[i], a0); a1 = MFMA(R[W2R(2 * i + 1)], uf[i], a1); }
        STAMP(3, 1 + 4 * ab);
        if (ab) BAR();
        red_write(lds, w, lane, a0, a1);
        BAR();
        const f32x4 v = red_sum(lds, w, lane);
        const u32x2 hr = ab ? hres1 : hres0;
        *(u32x2*)(out + (size_t)(rt * 32 + r) * DM + c7 * 64 + (w >> 2) * 32 + (w & 3) * 8 + 4 * hi) =
            u32x2{pk2(v[0] + bflo(hr.x), v[1] + bfhi(hr.x)), pk2(v[2] + bflo(hr.y), v[3] + bfhi(hr.y))};
        STAMP(3, 2 + 4 * ab);
    }
    if (b == 0 && tid == 0) flag_st(ws, FW_GEN, tag);
    STAMP_FLUSH();
    asm volatile(".Lfb_end:" ::: "memory");
}

extern "C" void kernel_launch(void* const* d_in, const int* in_sizes, int n_in, void* d_out, int out_size, void* d_ws, size_t ws_size, hipStream_t stream) {
    (void)hipFuncSetAttribute((const void*)fused_block, hipFuncAttributeMaxDynamicSharedMemorySize, LDS_BYTES);
    fused_block<<<256, 512, LDS_BYTES, stream>>>((const unsigned short*)d_in[0], (const unsigned short*)d_in[1], (const unsigned short*)d_in[2], (const unsigned short*)d_in[4],
                                                 (const unsigned short*)d_in[5], (char*)d_ws, (unsigned short*)d_out);
}
```

```cpp
#include <hip/hip_runtime.h>
#include <hip/hip_bf16.h>
#include <stdint.h>

typedef short bf16x8 __attribute__((ext_vector_type(8)));
typedef float f32x16 __attribute__((ext_vector_type(16)));
typedef float f32x4 __attribute__((ext_vector_type(4)));
typedef float f32x2 __attribute__((ext_vector_type(2)));
typedef unsigned u32x4 __attribute__((ext_vector_type(4)));
typedef unsigned u32x2 __attribute__((ext_vector_type(2)));
typedef __bf16 bf16x2_t __attribute__((ext_vector_type(2)));
typedef short v4i16_t __attribute__((ext_vector_type(4)));
#define LAS __attribute__((address_space(3)))
#define GAS __attribute__((address_space(1)))
#define DI __device__ __forceinline__
#define MFMA(a, b, c) __builtin_amdgcn_mfma_f32_32x32x16_bf16(__builtin_bit_cast(bf16x8, (a)), __builtin_bit_cast(bf16x8, (b)), (c), 0, 0, 0)
#define BAR() asm volatile("s_waitcnt lgkmcnt(0)\n\ts_barrier" ::: "memory")
#define VMBAR(N) asm volatile("s_waitcnt vmcnt(" #N ") lgkmcnt(0)\n\ts_barrier" ::: "memory")
#define VMWAIT(N) asm volatile("s_waitcnt vmcnt(" #N ")" ::: "memory")
#define SB() __builtin_amdgcn_sched_barrier(0)
#define PIN(v) asm volatile("" : "+v"(v))
#define RLX_AGENT __ATOMIC_RELAXED, __HIP_MEMORY_SCOPE_AGENT

constexpr int SEQ = 2048, DM = 448, NH = 16, DH = 28, FF = 1792;
constexpr float EPS = 1e-5f;
constexpr float LOG2E = 1.4426950408889634f;
constexpr float QSCALE = 0.18898223650461362f * LOG2E;
constexpr unsigned ONES = 0x3f803f80u;
constexpr int KB = 1024;
constexpr int FW_GEN = 0, FW_KV = 64, FW_W = FW_KV + 16 * 32, FW_RT = FW_W + 256;
constexpr int KV_OFF = 32 * KB;
constexpr int OF_OFF = KV_OFF + 16 * 64 * 4 * KB;
constexpr int WO_OFF = OF_OFF + 64 * 32 * KB;
constexpr int HF_OFF = WO_OFF + 14 * 32 * KB;
constexpr int W1_OFF = HF_OFF + 64 * 28 * KB;
constexpr int UF_OFF = W1_OFF + 56 * 28 * KB;
constexpr int W2_OFF = UF_OFF + 64 * 112 * KB;
constexpr int WS_BYTES = W2_OFF + 14 * 112 * KB;
constexpr int SC1 = 16;
static_assert((FW_RT + 3 * 64 * 32) * 4 <= KV_OFF, "flag block");

DI unsigned pk2(float lo, float hi) { f32x2 v = {lo, hi}; return __builtin_bit_cast(unsigned, __builtin_convertvector(v, bf16x2_t)); }
DI float dot2(unsigned a, unsigned b, float c) { return __builtin_amdgcn_fdot2_f32_bf16(__builtin_bit_cast(bf16x2_t, a), __builtin_bit_cast(bf16x2_t, b), c, false); }
DI float sum8(const u32x4 f, float c) { return dot2(f.w, ONES, dot2(f.z, ONES, dot2(f.y, ONES, dot2(f.x, ONES, c)))); }
DI float sq8(const u32x4 f, float c) { return dot2(f.w, f.w, dot2(f.z, f.z, dot2(f.y, f.y, dot2(f.x, f.x, c)))); }
DI float bflo(unsigned u) { return __uint_as_float(u << 16); }
DI float bfhi(unsigned u) { return __uint_as_float(u & 0xffff0000u); }
DI float swap_add(float v) { auto r = __builtin_amdgcn_permlane32_swap(__float_as_uint(v), __float_as_uint(v), false, false); return __uint_as_float(r[0]) + __uint_as_float(r[1]); }
DI float swap_max(float v) { auto r = __builtin_amdgcn_permlane32_swap(__float_as_uint(v), __float_as_uint(v), false, false); return fmaxf(__uint_as_float(r[0]), __uint_as_float(r[1])); }
DI void glds16(const void* sbase, unsigned voff, unsigned lds_base) {
    asm volatile("s_mov_b32 m0, %2\n\ts_nop 0\n\tglobal_load_lds_dwordx4 %0, %1" :: "v"(voff), "s"(sbase), "s"(lds_base) : "memory");
}
DI void glds16c(const void* sbase, unsigned voff, unsigned lds_base) {
    asm volatile("s_mov_b32 m0, %2\n\ts_nop 0\n\tglobal_load_lds_dwordx4 %0, %1 sc1" :: "v"(voff), "s"(sbase), "s"(lds_base) : "memory");
}
DI u32x2 trd(LAS const char* p) { return __builtin_bit_cast(u32x2, __builtin_amdgcn_ds_read_tr16_b64_v4i16((LAS v4i16_t*)p)); }
DI u32x4 trfrag(LAS const char* p, int second) { u32x2 a = trd(p), b = trd(p + second); return u32x4{a.x, a.y, b.x, b.y}; }
DI u32x4 pack8(const f32x16& a, int s) {
    return u32x4{pk2(a[8 * s], a[8 * s + 1]), pk2(a[8 * s + 2], a[8 * s + 3]), pk2(a[8 * s + 4], a[8 * s + 5]), pk2(a[8 * s + 6], a[8 * s + 7])};
}
DI float max16(const f32x16& p) {
    float a = fmaxf(fmaxf(p[0], p[1]), p[2]), b = fmaxf(fmaxf(p[3], p[4]), p[5]);
    a = fmaxf(fmaxf(a, p[6]), p[7]); b = fmaxf(fmaxf(b, p[8]), p[9]);
    a = fmaxf(fmaxf(a, p[10]), p[11]); b = fmaxf(fmaxf(b, p[12]), p[13]);
    a = fmaxf(fmaxf(a, p[14]), p[15]);
    return fmaxf(a, b);
}
DI u32x2 split3(float m) {
    const float v = -m;
    const unsigned u1 = __float_as_uint(v) & 0xffff0000u; const float r1 = v - __uint_as_float(u1);
    const unsigned u2 = __float_as_uint(r1) & 0xffff0000u; const float r2 = r1 - __uint_as_float(u2);
    const unsigned u3 = __float_as_uint(r2) & 0xffff0000u;
    return u32x2{(u1 >> 16) | u2, u3 >> 16};
}
DI u32x4 corr_a(float cs, int hi) {
    const unsigned h = pk2(cs, cs); const float lo = cs - bflo(h); const unsigned l = pk2(lo, lo);
    return hi ? u32x4{0u, 0u, 0u, 0u} : u32x4{h, l, 0u, 0u};
}
DI u32x4 corr_b(float mu) {
    const unsigned h = pk2(-mu, 0.f); const unsigned d = pk2(-mu, -mu - bflo(h));
    return u32x4{d, d, 0u, 0u};
}
DI u32x4 ldf(__amdgpu_buffer_rsrc_t rs, int l16, int soff) { return __builtin_amdgcn_raw_buffer_load_b128(rs, l16, soff, SC1); }
DI void stf(u32x4 v, __amdgpu_buffer_rsrc_t rs, int l16, int soff) {
    __builtin_amdgcn_raw_buffer_store_b128(v, rs, l16, soff, SC1);
    asm volatile("s_nop 1" :: "v"(v) : "memory");
}
DI unsigned flag_ld(char* ws, int word) { return __hip_atomic_load((GAS unsigned*)ws + word, RLX_AGENT); }
DI void flag_st(char* ws, int word, unsigned v) { __hip_atomic_store((GAS unsigned*)ws + word, v, RLX_AGENT); }
DI void poll_flags(char* ws, int word, unsigned tag, unsigned first) {
    unsigned v = first;
    for (unsigned spins = 0; !__all(v == tag) && spins < (1u << 17); ++spins) v = flag_ld(ws, word);
    __builtin_amdgcn_fence(__ATOMIC_ACQUIRE, "wavefront");
}

#define STAMP(kid, i)
#define STAMP_FLUSH()
constexpr int STAMP_BYTES = 0;

constexpr int A_XW = 16 * 1040, A_SLOT = A_XW + 8 * KB, A_WQ = A_SLOT, A_PT = A_WQ + 28 * KB + 64, A_CS = 146 * KB;
static_assert(6 * A_SLOT <= A_CS && A_PT + 16 * KB <= 3 * A_SLOT, "phase 1 LDS");
constexpr int B_RED = 0;
constexpr int B_HS = 64 * KB;
constexpr int B_QS = 76 * KB;
constexpr int B_ST = 92 * KB;
constexpr int B_W1 = 96 * KB;
constexpr int B_FLAG = 152 * KB;
constexpr int STAMP_LDS = B_FLAG + 64;
constexpr int LDS_BYTES = STAMP_LDS + STAMP_BYTES;
static_assert(LDS_BYTES <= 160 * KB, "LDS");

DI void red_write(LAS char* lds, int w, int lane, const f32x16& a, const f32x16& c) {
    LAS f32x4* red = (LAS f32x4*)(lds + B_RED) + (w * 8) * 64 + lane;
#pragma unroll
    for (int g = 0; g < 4; ++g) {
        red[g * 64] = f32x4{a[4 * g], a[4 * g + 1], a[4 * g + 2], a[4 * g + 3]};
        red[(4 + g) * 64] = f32x4{c[4 * g], c[4 * g + 1], c[4 * g + 2], c[4 * g + 3]};
    }
}
DI f32x4 red_sum(LAS char* lds, int w, int lane) {
    LAS const f32x4* red = (LAS const f32x4*)(lds + B_RED) + w * 64 + lane;
    f32x4 v[8];
#pragma unroll
    for (int i = 0; i < 8; ++i) v[i] = red[i * 8 * 64];
    SB();
    return ((v[0] + v[1]) + (v[2] + v[3])) + ((v[4] + v[5]) + (v[6] + v[7]));
}

__global__ __launch_bounds__(512, 2) void fused_block(const unsigned short* __restrict__ x, const unsigned short* __restrict__ wqkv, const unsigned short* __restrict__ wo,
                                                      const unsigned short* __restrict__ w1, const unsigned short* __restrict__ w2, char* __restrict__ ws, unsigned short* __restrict__ out) {
    extern __shared__ __attribute__((aligned(16))) char smem[];
    const int tid = threadIdx.x, lane = tid & 63, w = __builtin_amdgcn_readfirstlane(tid >> 6), r = lane & 31, hi = lane >> 5;
    const int b = blockIdx.x, xcd = b & 7, bi = b >> 3;
    unsigned sink;
    {
        unsigned long long pc; unsigned len;
        asm volatile(".Lfb_begin:\n\ts_getpc_b64 %0\n\ts_mov_b32 %1, .Lfb_end-.Lfb_begin" : "=s"(pc), "=s"(len));
        const unsigned off = (unsigned)(((lane & 1) * 8 + w) * 32 + bi) * 128u;
        asm volatile("global_load_dword %0, %1, %2" : "=v"(sink) : "v"(off < len && len < 65536u ? off : 0u), "s"(pc) : "memory");
    }
    asm volatile("" :: "s"(x), "s"(wqkv), "s"(ws));
    LAS char* lds = (LAS char*)smem;
    const unsigned lds0 = (unsigned)(uintptr_t)smem;
    const int h = (xcd & 3) * 4 + (bi & 3), rb = (xcd >> 2) * 8 + (bi >> 2);
    const int rot = (2 * (bi & 3) + (bi >> 2)) % 7;
    const int l16 = lane * 16;
    const int trq = (lane & 15) >> 2, trc = ((lane >> 4) & 1) * 32 + (lane & 3) * 8;
    const __amdgpu_buffer_rsrc_t rs = __builtin_amdgcn_make_buffer_rsrc((void*)ws, 0, WS_BYTES, 0x00020000);
    LAS float* csl = (LAS float*)(lds + A_CS);
    volatile LAS unsigned* lflag = (volatile LAS unsigned*)(lds + B_FLAG);
    STAMP(0, 0);
    unsigned tag;
    u32x4 q0 = {}, q1 = {};
    if (__builtin_expect(w >= 4, 1)) {
        const int L = w - 4;
        const char* xb = (const char*)x + (size_t)(rb * 128 + 64 * (L >> 1) + 4 * (L & 1)) * 896;
        const unsigned xo0 = (lane >> 3) * (8 * 896) + (lane & 7) * 16, xo1 = xo0 + 896, xo2 = xo0 + 2 * 896, xo3 = xo0 + 3 * 896;
        const char* wkv = (const char*)wqkv + (size_t)(16 * L) * 2688 + 896 + 112 * h;
        const unsigned woff = (lane >> 3) * 2688 + (lane & 7) * 16, woff2 = woff + 8 * 2688;
#pragma unroll 1
        for (int s = 0; s < 7; ++s) {
            int kb = s + rot; kb = kb >= 7 ? kb - 7 : kb;
            const unsigned d_ = lds0 + (s == 6 ? 0 : s) * A_SLOT;
            if (s == 6) { while (lflag[4] < 4u) __builtin_amdgcn_s_sleep(1); }
            glds16(xb + kb * 128, xo0, d_ + L * 4160); glds16(xb + kb * 128, xo1, d_ + L * 4160 + 1040);
            glds16(xb + kb * 128, xo2, d_ + L * 4160 + 2080); glds16(xb + kb * 128, xo3, d_ + L * 4160 + 3120);
            if ((lane & 7) != 7) { glds16(wkv + (size_t)kb * (64 * 2688), woff, d_ + A_XW + L * 2048); glds16(wkv + (size_t)kb * (64 * 2688), woff2, d_ + A_XW + L * 2048 + 1024); }
            if (s == 0) BAR();
            else { VMWAIT(6); if (lane == 0) __hip_atomic_fetch_add((LAS unsigned*)&lflag[8 + s - 1], 1u, __ATOMIC_RELAXED, __HIP_MEMORY_SCOPE_WORKGROUP); }
        }
        VMWAIT(0);
        if (lane == 0) __hip_atomic_fetch_add((LAS unsigned*)&lflag[8 + 6], 1u, __ATOMIC_RELAXED, __HIP_MEMORY_SCOPE_WORKGROUP);
        PIN(sink);
        while (lflag[5] < 4u) __builtin_amdgcn_s_sleep(1);
        {
            const char* wq = (const char*)wqkv + (size_t)(16 * L) * 2688 + ((56 * h) & ~15);
            const unsigned qoff = (lane >> 2) * 2688 + (lane & 3) * 16;
#pragma unroll 1
            for (int i = 0; i < 7; ++i) { int kb = i + rot; kb = kb >= 7 ? kb - 7 : kb; glds16(wq + (size_t)kb * (64 * 2688), qoff, lds0 + A_WQ + (4 * i + L) * 1024); }
        }
        const char* psrc; int ppitch, pdst, pstep, k0;
        if (b < 112) { const int cp = b >> 2; k0 = (b & 3) * 7; psrc = (const char*)w1 + cp * 128; ppitch = FF * 2; pdst = W1_OFF + (cp * 2 * 28) * KB; pstep = 28 * KB; }
        else if (b < 224) { const int q = b - 112, cp = q >> 4; k0 = (q & 15) * 7; psrc = (const char*)w2 + cp * 128; ppitch = DM * 2; pdst = W2_OFF + (cp * 2 * 112) * KB; pstep = 112 * KB; }
        else { k0 = (b - 224) * 7; psrc = (const char*)wo; ppitch = DM * 2; pdst = WO_OFF; pstep = 32 * KB; }
        int pks[2], pd[2];
#pragma unroll
        for (int i = 0; i < 2; ++i) {
            const int kk = k0 + ((L + 4 * i < 7) ? L + 4 * i : L);
            const int cp = kk >> 5, ks = kk & 31;
            pks[i] = (b >= 224) ? (ks & 1) : 0; pd[i] = (b >= 224) ? pdst + (cp * 64 + ks) * KB : pdst + kk * KB;
#pragma unroll
            for (int u = 0; u < 2; ++u) {
                const int row = 8 * u + (lane >> 3), c = 16 * (ks & 1) + row;
                const int srow = (b >= 224) ? (ks >> 1) * DH + (c < DH ? c : DH - 1) : 16 * kk + row;
                glds16(psrc, (unsigned)(srow * ppitch + ((b >= 224) ? cp * 128 : 0) + (lane & 7) * 16), lds0 + A_PT + ((L * 2 + i) * 2 + u) * 1024);
            }
        }
        VMWAIT(4);
        float cq = 0.f;
        LAS const char* tq = lds + A_WQ + L * 1024 + (8 * hi + trq) * 64 + trc + ((56 * h) & 15);
#pragma unroll
        for (int i = 0; i < 7; ++i) cq = sum8(trfrag(tq + i * 4096, 256), cq);
        csl[(2 * 32 + r) * 8 + 2 * L + hi] = cq;
        STAMP(0, 4);
        BAR();
        tag = __builtin_amdgcn_readfirstlane(lflag[2]);
        LAS const char* tp = lds + A_PT + L * 4096 + (4 * hi + trq) * 128 + trc;
        VMWAIT(0);
#pragma unroll
        for (int i = 0; i < 2; ++i) {
            if (L + 4 * i < 7) {
#pragma unroll
                for (int ct = 0; ct < 2; ++ct) {
                    u32x4 f = trfrag(tp + i * 2048 + ct * 64, 1024);
                    if (pks[i] && hi) { f.z = 0u; f.w = 0u; }
                    stf(f, rs, l16, pd[i] + ct * pstep);
                }
            }
        }
        STAMP(0, 5);
    } else {
        const int rt = rb * 4 + w;
        const unsigned gen = flag_ld(ws, FW_GEN);
        if (tid < 16) lflag[tid] = 0u;
        if ((lane & 7) == 7) {
#pragma unroll
            for (int s = 0; s < 12; ++s) *(LAS u32x4*)(lds + (s >> 1) * A_SLOT + A_XW + w * 2048 + (s & 1) * 1024 + l16) = u32x4{ONES, ONES, ONES, ONES};
        }
        BAR();
        LAS const char* xr = lds + (8 * (w >> 1) + (r & 7)) * 1040 + (4 * (w & 1) + (r >> 3)) * 128 + hi * 16;
        LAS const char* tw = lds + A_XW + (8 * hi + trq) * 128 + trc;
        u32x4 xf[28];
        f32x16 a0 = {}, a1 = {};
        float s2 = 0.f, c0 = 0.f, c1 = 0.f;
        u32x4 wa[2][4], wb[2][4];
#define A_READ(s) { while (lflag[8 + (s)] < 4u) {} \
        asm volatile("" ::: "memory"); \
        _Pragma("unroll") for (int i = 0; i < 4; ++i) { \
            xf[4 * (s) + i] = *(LAS const u32x4*)(xr + ((s) % 6) * A_SLOT + (i ^ w) * 32); \
            wa[(s) & 1][i] = trfrag(tw + ((s) % 6) * A_SLOT + (i ^ w) * 2048, 512); wb[(s) & 1][i] = trfrag(tw + ((s) % 6) * A_SLOT + (i ^ w) * 2048 + 56, 512); } \
        SB(); }
#define A_MM(s, i) { a0 = MFMA(wa[(s) & 1][i], xf[4 * (s) + i], a0); a1 = MFMA(wb[(s) & 1][i], xf[4 * (s) + i], a1); PIN(a0); PIN(a1); SB(); }
#define A_STAGE(s, DONE) { A_MM(s, 0) A_MM(s, 1) \
        if ((DONE) && lane == 0) { asm volatile("s_waitcnt lgkmcnt(0)" ::: "memory"); __hip_atomic_fetch_add((LAS unsigned*)&lflag[DONE], 1u, __ATOMIC_RELAXED, __HIP_MEMORY_SCOPE_WORKGROUP); } \
        if ((s) < 6) A_READ(((s) < 6 ? (s) + 1 : 6)) \
        A_MM(s, 2) A_MM(s, 3) \
        _Pragma("unroll") for (int i = 0; i < 4; ++i) s2 = sq8(xf[4 * (s) + i], s2); \
        c0 = sum8(wa[(s) & 1][0], c0); c1 = sum8(wb[(s) & 1][0], c1); PIN(s2); PIN(c0); PIN(c1); SB(); }
        A_READ(0)
        A_STAGE(0, 4)
        STAMP(0, 1);
        A_STAGE(1, 0) A_STAGE(2, 5) A_STAGE(3, 0)
        STAMP(0, 2);
        A_STAGE(4, 0) A_STAGE(5, 0)
        csl[(0 * 32 + r) * 8 + 2 * w + hi] = c0; csl[(1 * 32 + r) * 8 + 2 * w + hi] = c1;
        asm volatile("s_waitcnt lgkmcnt(0)" ::: "memory");
        if (lane == 0) __hip_atomic_fetch_add((LAS unsigned*)&lflag[6], 1u, __ATOMIC_RELAXED, __HIP_MEMORY_SCOPE_WORKGROUP);
        c0 = 0.f; c1 = 0.f;
        A_STAGE(6, 0)
#pragma unroll
        for (int i = 1; i < 4; ++i) { c0 = sum8(wa[0][i], c0); c1 = sum8(wb[0][i], c1); }
        STAMP(0, 3);
        tag = __builtin_amdgcn_readfirstlane(gen) + 1u;
        PIN(sink);
        if (tid == 0) lflag[2] = tag;
        s2 = swap_add(s2);
        const float mu = swap_add(hi ? a1[12] : 0.f) * (1.f / DM), var = fmaxf(s2 * (1.f / DM) - mu * mu, 0.f), rstd = __builtin_amdgcn_rsqf(var + EPS);
        const u32x4 cb = corr_b(mu);
        const int dst = KV_OFF + (((2 * h) & 15) * 64 + rt) * 4 * KB + (h < 8 ? 0 : 2 * KB);
        while (lflag[6] < 4u) {}
        asm volatile("" ::: "memory");
#define A_CS(t) ({ const f32x4 p0 = *(LAS const f32x4*)(csl + ((t) * 32 + r) * 8), p1 = *(LAS const f32x4*)(csl + ((t) * 32 + r) * 8 + 4); ((p0.x + p0.y) + (p0.z + p0.w)) + ((p1.x + p1.y) + (p1.z + p1.w)); })
        const float cs0 = A_CS(0) + swap_add(c0), cs1 = A_CS(1) + swap_add(c1);
        a0 = MFMA(corr_a(cs0, hi), cb, a0); a1 = MFMA(corr_a(cs1, hi), cb, a1);
        a0 *= rstd; a1 *= rstd;
        u32x4 f00 = pack8(a0, 0), f01 = pack8(a0, 1), f10 = pack8(a1, 0), f11 = pack8(a1, 1);
        const unsigned pz = h < 8 ? ONES : 0x00003f80u, pw = h < 8 ? 0x00003f80u : 0u;
        if (hi) { f01.z = pz; f01.w = pw; f11.z = pz; f11.w = pw; }
        if (h >= 8) {
            LAS char* tb = lds + 4 * A_SLOT + w * 4096;
            LAS char* wr = tb + r * 64 + hi * 8;
            *(LAS u32x2*)(wr) = u32x2{f00.x, f00.y}; *(LAS u32x2*)(wr + 16) = u32x2{f00.z, f00.w}; *(LAS u32x2*)(wr + 32) = u32x2{f01.x, f01.y}; *(LAS u32x2*)(wr + 48) = u32x2{f01.z, f01.w};
            *(LAS u32x2*)(wr + 2048) = u32x2{f10.x, f10.y}; *(LAS u32x2*)(wr + 2064) = u32x2{f10.z, f10.w}; *(LAS u32x2*)(wr + 2080) = u32x2{f11.x, f11.y}; *(LAS u32x2*)(wr + 2096) = u32x2{f11.z, f11.w};
            asm volatile("s_waitcnt lgkmcnt(0)" ::: "memory");
            LAS const char* rd = tb + (4 * hi + trq) * 64 + trc;
            f00 = trfrag(rd, 512); f01 = trfrag(rd + 1024, 512); f10 = trfrag(rd + 2048, 512); f11 = trfrag(rd + 3072, 512);
        }
        stf(f00, rs, l16, dst); stf(f01, rs, l16, dst + KB); stf(f10, rs, l16, dst + 64 * 4 * KB); stf(f11, rs, l16, dst + 64 * 4 * KB + KB);
        STAMP(0, 4);
        VMWAIT(0);
        if (lane == 0 && __hip_atomic_fetch_add((LAS unsigned*)&lflag[7], 1u, __ATOMIC_RELAXED, __HIP_MEMORY_SCOPE_WORKGROUP) == 3u) flag_st(ws, FW_KV + h * 32 + rb, tag);
        BAR();
        LAS const char* tq = lds + A_WQ + (8 * hi + trq) * 64 + trc + ((56 * h) & 15);
        f32x16 aq = {};
        u32x4 wq[8];
#pragma unroll
        for (int i = 0; i < 8; ++i) wq[i] = trfrag(tq + (i >> 2) * 4096 + ((i & 3) ^ w) * 1024, 256);
        SB();
#pragma unroll
        for (int i = 0; i < 28; ++i) {
            aq = MFMA(wq[i & 7], xf[i], aq); SB();
            if (i + 8 < 28) wq[i & 7] = trfrag(tq + ((i + 8) >> 2) * 4096 + ((i & 3) ^ w) * 1024, 256);
            SB();
        }
        aq = MFMA(corr_a(A_CS(2), hi), cb, aq);
        aq *= rstd * QSCALE;
        q0 = pack8(aq, 0); q1 = pack8(aq, 1);
        if (hi) { q1.z = 0u; q1.w = 0u; }
        STAMP(0, 5);
    }
    const bool post = bi < 28;
    const int gam = 4 * xcd + bi / 7, c7 = bi % 7;
    const int rtA = (gam >> 1) * 4 + (gam & 1);
    const __amdgpu_buffer_rsrc_t rw = __builtin_amdgcn_make_buffer_rsrc((void*)ws, 0, post ? WS_BYTES : 0, 0x00020000);
    u32x4 R[32];
    const int kvo = KV_OFF + (h * 64 + w * 8) * 4 * KB;
#define KVA(i) (kvo + ((((i) + 4 * rb) & 31) * KB))
    {
        const int fw = FW_KV + ((h >> 1) + 8 * (lane & 1)) * 32 + 2 * w + ((lane >> 1) & 1);
        poll_flags(ws, fw, tag, tag + 1u);
        STAMP(0, 6);
#pragma unroll
        for (int i = 0; i < 8; ++i) { R[i] = ldf(rs, l16, KVA(i)); SB(); }
        if (w < 4) {
            f32x16 sd = {};
            sd = MFMA(R[0], q0, sd); sd = MFMA(R[1], q1, sd);
            const float m = swap_max(max16(sd));
            if (hi) { const u32x2 p = split3(m); q1.z = p.x; q1.w = p.y; }
            *(LAS u32x4*)(lds + B_QS + w * 2048 + l16) = q0; *(LAS u32x4*)(lds + B_QS + w * 2048 + 1024 + l16) = q1;
        }
    }
    BAR();
    if (tid == 256) flag_st(ws, FW_W + b, tag);
    STAMP(0, 7);
    const int woo = WO_OFF + (2 * c7 * 32 + 4 * w) * KB, w1o = W1_OFF + (8 * c7 + w) * 28 * KB, w2o = W2_OFF + (2 * c7 * 112 + 14 * w) * KB;
#define NEXTW(i) ((i) < 4 ? woo + (i) * KB : (i) < 8 ? woo + (28 + (i)) * KB : w1o + ((i) - 8) * KB)
#define EXP4(S, i) { S[i] = __builtin_amdgcn_exp2f(S[i]); S[i + 1] = __builtin_amdgcn_exp2f(S[i + 1]); S[i + 2] = __builtin_amdgcn_exp2f(S[i + 2]); S[i + 3] = __builtin_amdgcn_exp2f(S[i + 3]); }
#define HOOK(PASS, i) if ((i) >= 32) { glds16c(ws + w1o + (16 + (i) - 32) * KB, l16, lds0 + B_W1 + (w * 7 + (i) - 32) * KB); } \
        else if ((i) >= 0) R[(i) & 31] = (PASS) ? ldf(rw, l16, NEXTW((i) & 31)) : ldf(rs, l16, KVA((i) & 31));
#define STEP(PASS, C, N, OC, OP, QN0, QN1, t, tn, tp, FIRST, LAST, L0, L1, L2, L3) { \
        if (!(LAST)) { N = MFMA(R[4 * (tn)], QN0, zero16); SB(); } \
        HOOK(PASS, L0) \
        EXP4(C, 0) p0.x = pk2(C[0], C[1]); p0.y = pk2(C[2], C[3]); PIN(p0); SB(); \
        if (!(FIRST)) { OP = MFMA(R[4 * (tp) + 3], p1, OP); SB(); } \
        HOOK(PASS, L1) \
        EXP4(C, 4) p0.z = pk2(C[4], C[5]); p0.w = pk2(C[6], C[7]); PIN(p0); SB(); \
        if (!(LAST)) { N = MFMA(R[4 * (tn) + 1], QN1, N); SB(); } \
        HOOK(PASS, L2) \
        EXP4(C, 8) p1.x = pk2(C[8], C[9]); p1.y = pk2(C[10], C[11]); PIN(p1); SB(); \
        OC = MFMA(R[4 * (t) + 2], p0, OC); SB(); \
        HOOK(PASS, L3) \
        EXP4(C, 12) p1.z = pk2(C[12], C[13]); p1.w = pk2(C[14], C[15]); PIN(p1); SB(); }
#define TILE(PASS, t, A0, A1, A2, A3, B0, B1, B2, B3) \
        STEP(PASS, sa, sb, oa, ob, qb0, qb1, t, t, (t) - 1 < 0 ? 0 : (t) - 1, (t) == 0, false, A0, A1, A2, A3) \
        STEP(PASS, sb, sa, ob, oa, qa0, qa1, t, (t) + 1 > 7 ? 7 : (t) + 1, t, false, (t) == 7, B0, B1, B2, B3)
#define TILE0(t) TILE(0, t, 4 * (t) + 8, -1, 4 * (t) + 9, -1, 4 * (t) + 10, -1, 4 * (t) + 11, -1)
#define COMBINE(PASS) { \
        LAS const float* lp = (LAS const float*)(lds + B_RED) + (((w >> 2) * 4 + 3) * 64 + 32 + r) * 4; \
        float lv[8]; _Pragma("unroll") for (int i = 0; i < 8; ++i) lv[i] = lp[i * 8 * 64 * 4]; \
        const f32x4 o = red_sum(lds, w, lane); \
        const float il = __builtin_amdgcn_rcpf(((lv[0] + lv[1]) + (lv[2] + lv[3])) + ((lv[4] + lv[5]) + (lv[6] + lv[7]))); \
        const int qt = rb * 4 + (PASS) * 2 + (w >> 2); \
        __builtin_amdgcn_raw_buffer_store_b64(u32x2{pk2(o[0] * il, o[1] * il), pk2(o[2] * il, o[3] * il)}, rs, l16 + (w & 1) * 8, OF_OFF + ((qt * 16 + h) * 2 + ((w >> 1) & 1)) * KB, SC1); }
#define FLAG_O(PASS) { if (tid < 2) flag_st(ws, FW_RT + (0 * 64 + rb * 4 + (PASS) * 2 + tid) * 32 + h, tag); }
#define LOADQ(PASS) \
        qa0 = *(LAS const u32x4*)(lds + B_QS + (PASS) * 4096 + l16); qa1 = *(LAS const u32x4*)(lds + B_QS + (PASS) * 4096 + 1024 + l16); \
        qb0 = *(LAS const u32x4*)(lds + B_QS + (PASS) * 4096 + 2048 + l16); qb1 = *(LAS const u32x4*)(lds + B_QS + (PASS) * 4096 + 3072 + l16);
#define PASS_BEGIN(PASS) LOADQ(PASS) oa = zero16; ob = zero16; sa = MFMA(R[0], qa0, zero16); sa = MFMA(R[1], qa1, sa); SB();
#define PASS_END(PASS) \
        ob = MFMA(R[31], p1, ob); \
        { const bool bad = hi && !(oa[12] < 1.0e18f && ob[12] < 1.0e18f); \
          if (__any(bad) && lane == 0) lflag[PASS] = 1u; } \
        red_write(lds, w, lane, oa, ob);
    u32x2 xres0, xres1;
    unsigned pfo;
    {
        const f32x16 zero16 = {};
        u32x4 qa0, qa1, qb0, qb1, p0, p1; f32x16 oa, ob, sa, sb;
        PASS_BEGIN(0)
        TILE0(0) TILE0(1) TILE0(2) TILE0(3) TILE0(4) TILE0(5)
        TILE(0, 6, -1, -1, -1, -1, -1, -1, -1, -1)
        const u32x4 wfl = __builtin_amdgcn_raw_buffer_load_b128(rs, l16, FW_W * 4, SC1);
        TILE(0, 7, -1, -1, -1, -1, -1, -1, -1, -1)
        STAMP(1, 0);
        PASS_END(0)
        {
            u32x4 v = wfl;
            for (unsigned spins = 0; !__all(v.x == tag && v.y == tag && v.z == tag && v.w == tag) && spins < (1u << 17); ++spins) {
                asm volatile("" ::: "memory");
                v = __builtin_amdgcn_raw_buffer_load_b128(rs, l16, FW_W * 4, SC1);
            }
            __builtin_amdgcn_fence(__ATOMIC_ACQUIRE, "wavefront");
        }
        PASS_BEGIN(1)
        TILE(1, 0, 0, 32, 1, 33, 34, 35, 36, 2)
        TILE(1, 1, 4, 3, 5, 37, 38, -1, -1, 6)
        BAR();
        STAMP(1, 1);
        const bool okA = lflag[0] == 0u;
        if (okA) COMBINE(0)
        TILE(1, 2, -1, -1, -1, -1, -1, -1, -1, -1)
        VMBAR(0);
        if (okA) FLAG_O(0)
        STAMP(1, 2);
        TILE(1, 3, 7, 8, 9, 10, 11, 12, 13, 14)
        TILE(1, 4, 16, 15, 17, -1, -1, -1, -1, 18)
        TILE(1, 5, 20, 19, 21, -1, -1, -1, -1, 22)
        {
            const unsigned short* xp = x + (size_t)((post ? rtA : 0) * 32 + r) * DM + c7 * 64 + (w >> 2) * 32 + (w & 3) * 8 + 4 * hi;
            xres0 = *(const u32x2*)xp; xres1 = *(const u32x2*)(xp + 64 * DM);
        }
        TILE(1, 6, -1, 23, -1, -1, -1, -1, -1, -1)
        pfo = flag_ld(ws, FW_RT + (0 * 64 + (post ? rtA : 0)) * 32 + 2 * w + (lane & 1));
        TILE(1, 7, -1, -1, -1, -1, -1, -1, -1, -1)
        STAMP(1, 3);
        PASS_END(1)
        BAR();
        const bool fb = lflag[0] != 0u || lflag[1] != 0u;
        if (__builtin_expect(fb, 0)) {
            if (lflag[1] == 0u) COMBINE(1)
#pragma unroll 1
            for (int p = 0; p < 2; ++p) {
                if (lflag[p] == 0u) continue;
                LOADQ(p)
                float ma = -INFINITY, mb = -INFINITY;
#pragma unroll 1
                for (int t = 0; t < 8; ++t) {
                    const u32x4 k0 = ldf(rs, l16, kvo + t * 4 * KB), k1 = ldf(rs, l16, kvo + t * 4 * KB + KB);
                    sa = MFMA(k0, qa0, zero16); sa = MFMA(k1, qa1, sa); sb = MFMA(k0, qb0, zero16); sb = MFMA(k1, qb1, sb);
                    ma = fmaxf(ma, max16(sa)); mb = fmaxf(mb, max16(sb));
                }
                ma = swap_max(ma); mb = swap_max(mb);
                LAS float* mx = (LAS float*)(lds + B_ST);
                mx[w * 64 + lane] = hi ? mb : ma;
                BAR();
#pragma unroll
                for (int i = 0; i < 8; ++i) { ma = fmaxf(ma, mx[i * 64 + r]); mb = fmaxf(mb, mx[i * 64 + 32 + r]); }
                if (hi) {
                    const u32x2 pa = split3(ma - (bflo(qa1.z) + bfhi(qa1.z) + bflo(qa1.w))), pb = split3(mb - (bflo(qb1.z) + bfhi(qb1.z) + bflo(qb1.w)));
                    qa1.z = pa.x; qa1.w = pa.y; qb1.z = pb.x; qb1.w = pb.y;
                }
                oa = zero16; ob = zero16;
#pragma unroll 1
                for (int t = 0; t < 8; ++t) {
                    const u32x4 k0 = ldf(rs, l16, kvo + t * 4 * KB), k1 = ldf(rs, l16, kvo + t * 4 * KB + KB), v0 = ldf(rs, l16, kvo + t * 4 * KB + 2 * KB), v1 = ldf(rs, l16, kvo + t * 4 * KB + 3 * KB);
                    sa = MFMA(k0, qa0, zero16); sa = MFMA(k1, qa1, sa); sb = MFMA(k0, qb0, zero16); sb = MFMA(k1, qb1, sb);
                    EXP4(sa, 0) EXP4(sa, 4) EXP4(sa, 8) EXP4(sa, 12) EXP4(sb, 0) EXP4(sb, 4) EXP4(sb, 8) EXP4(sb, 12)
                    oa = MFMA(v0, pack8(sa, 0), oa); oa = MFMA(v1, pack8(sa, 1), oa); ob = MFMA(v0, pack8(sb, 0), ob); ob = MFMA(v1, pack8(sb, 1), ob);
                }
                red_write(lds, w, lane, oa, ob);
                BAR();
                COMBINE(p)
                VMBAR(0);
                if (p == 0) FLAG_O(0)
            }
        }
        if (post) {
            poll_flags(ws, FW_RT + (0 * 64 + rtA) * 32 + 2 * w + (lane & 1), tag, pfo);
#pragma unroll
            for (int i = 0; i < 4; ++i) R[24 + i] = ldf(rs, l16, OF_OFF + (rtA * 32 + 4 * w + i) * KB);
        }
        if (!fb) COMBINE(1)
    }
    if (!post) { VMBAR(0); FLAG_O(1) STAMP_FLUSH(); return; }
    const int rtB = rtA + 2;
    const int ou = l16 + (w & 1) * 8;
    u32x4 E[5], of[4];
    f32x16 a0, a1;
#define WO_LOAD(rt) { _Pragma("unroll") for (int i = 0; i < 4; ++i) of[i] = ldf(rs, l16, OF_OFF + ((rt) * 32 + 4 * w + i) * KB); SB(); }
#define WO_MFMA() { a0 = f32x16{}; a1 = f32x16{}; _Pragma("unroll") for (int i = 0; i < 4; ++i) { a0 = MFMA(R[i], of[i], a0); a1 = MFMA(R[4 + i], of[i], a1); } }
#define WO_FIN(rt, xr, hr) { const f32x4 v = red_sum(lds, w, lane); \
        hr = u32x2{pk2(v[0] + bflo(xr.x), v[1] + bfhi(xr.x)), pk2(v[2] + bflo(xr.y), v[3] + bfhi(xr.y))}; \
        __builtin_amdgcn_raw_buffer_store_b64(hr, rs, ou, HF_OFF + ((rt) * 28 + c7 * 4 + (w >> 1)) * KB, SC1); }
#define ROT14(k) ((k) + 2 * c7 >= 14 ? (k) + 2 * c7 - 14 : (k) + 2 * c7)
#define W2R(i) ((i) < 8 ? 24 + (i) : (i) - 8)
#define W2_LOAD(lo, hi_) { _Pragma("unroll") for (int i = (lo); i < (hi_); ++i) R[W2R(i)] = ldf(rs, l16, w2o + (ROT14(i >> 1) + (i & 1) * 112) * KB); SB(); }
    u32x2 hres0, hres1;
#pragma unroll
    for (int i = 0; i < 4; ++i) of[i] = R[24 + i];
    WO_MFMA()
    VMBAR(0);
    FLAG_O(1)
    STAMP(1, 4);
#pragma unroll
    for (int i = 0; i < 5; ++i) E[i] = ldf(rs, l16, w1o + (23 + i) * KB);
    W2_LOAD(0, 4)
    red_write(lds, w, lane, a0, a1);
    BAR();
    WO_FIN(rtA, xres0, hres0)
    STAMP(1, 5);
    float cs1 = 0.f;
    LAS const char* w1l = lds + B_W1 + w * 7 * KB + l16;
#pragma unroll
    for (int i = 0; i < 16; ++i) cs1 = sum8(R[8 + i], cs1);
#pragma unroll
    for (int i = 0; i < 7; ++i) cs1 = sum8(*(LAS const u32x4*)(w1l + i * KB), cs1);
    PIN(cs1); SB();
    poll_flags(ws, FW_RT + (0 * 64 + rtB) * 32 + 2 * w + (lane & 1), tag, tag + 1u);
    WO_LOAD(rtB)
    BAR();
    if (tid == 0) flag_st(ws, FW_RT + (1 * 64 + rtA) * 32 + c7, tag);
    STAMP(1, 6);
    W2_LOAD(4, 8)
    WO_MFMA()
    red_write(lds, w, lane, a0, a1);
    BAR();
    WO_FIN(rtB, xres1, hres1)
    STAMP(1, 7);
#pragma unroll
    for (int i = 0; i < 5; ++i) cs1 = sum8(E[i], cs1);
    const u32x4 ca = corr_a(swap_add(cs1), hi);
    const int wp = w < 7 ? w : 6;
#define H_LOAD(rt) { _Pragma("unroll") for (int i = 0; i < 4; ++i) of[i] = ldf(rs, l16, HF_OFF + ((rt) * 28 + 4 * wp + i) * KB); SB(); }
#define H_STAGE(hs, ab) { float s1 = 0.f, s2 = 0.f; \
        _Pragma("unroll") for (int i = 0; i < 4; ++i) { *(LAS u32x4*)(lds + (hs) + l16 + (4 * wp + i) * KB) = of[i]; s1 = sum8(of[i], s1); s2 = sq8(of[i], s2); } \
        s1 = swap_add(s1); s2 = swap_add(s2); ((LAS f32x2*)(lds + B_ST))[(ab) * 224 + wp * 32 + r] = f32x2{s1, s2}; }
#define F1_MFMA(hs, HOOK) { u32x4 hb[4], wl[4]; a0 = f32x16{}; \
        _Pragma("unroll") for (int i = 0; i < 4; ++i) hb[i] = *(LAS const u32x4*)(lds + (hs) + l16 + i * KB); \
        SB(); \
        _Pragma("unroll") for (int i = 0; i < 28; ++i) { \
            a0 = MFMA(i < 16 ? R[8 + (i < 16 ? i : 0)] : i < 23 ? wl[i & 3] : E[i < 23 ? 0 : i - 23], hb[i & 3], a0); SB(); \
            if (i + 4 < 28) hb[i & 3] = *(LAS const u32x4*)(lds + (hs) + l16 + (i + 4) * KB); \
            if (i + 4 >= 16 && i + 4 < 23) wl[i & 3] = *(LAS const u32x4*)(w1l + (i + 4 - 16) * KB); \
            HOOK; SB(); } }
#define F1_FIN(rt, ab) { LAS const f32x2* st = (LAS const f32x2*)(lds + B_ST) + (ab) * 224 + r; f32x2 sv = st[0]; \
        _Pragma("unroll") for (int i = 1; i < 7; ++i) sv += st[i * 32]; \
        const float mu = sv.x * (1.f / DM), var = fmaxf(sv.y * (1.f / DM) - mu * mu, 0.f), rstd = __builtin_amdgcn_rsqf(var + EPS); \
        a0 = MFMA(ca, corr_b(mu), a0); \
        const float nr = -LOG2E * rstd; \
        _Pragma("unroll") for (int i = 0; i < 16; ++i) a0[i] = a0[i] * rstd * __builtin_amdgcn_rcpf(1.f + __builtin_amdgcn_exp2f(a0[i] * nr)); \
        const int ud = UF_OFF + ((rt) * 112 + (8 * c7 + w) * 2) * KB; \
        stf(pack8(a0, 0), rs, l16, ud); stf(pack8(a0, 1), rs, l16, ud + KB); }
    poll_flags(ws, FW_RT + (1 * 64 + rtA) * 32 + wp, tag, tag + 1u);
    H_LOAD(rtA)
    H_STAGE(B_HS, 0)
    BAR();
    if (tid == 0) flag_st(ws, FW_RT + (1 * 64 + rtB) * 32 + c7, tag);
    STAMP(2, 0);
    unsigned pf;
    F1_MFMA(B_HS, if (i < 8) R[i] = ldf(rs, l16, w2o + (ROT14((i + 8) >> 1) + (i & 1) * 112) * KB); if (i == 20) pf = flag_ld(ws, FW_RT + (1 * 64 + rtB) * 32 + wp))
    STAMP(2, 1);
    poll_flags(ws, FW_RT + (1 * 64 + rtB) * 32 + wp, tag, pf);
    H_LOAD(rtB)
    STAMP(2, 2);
    F1_FIN(rtA, 0)
    H_STAGE(B_RED, 1)
    STAMP(2, 3);
    VMBAR(0);
    if (tid == 0) flag_st(ws, FW_RT + (2 * 64 + rtA) * 32 + c7, tag);
    STAMP(2, 4);
    F1_MFMA(B_RED, if (i < 12) R[8 + (i < 12 ? i : 0)] = ldf(rs, l16, w2o + (ROT14(((i < 12 ? i : 0) + 16) >> 1) + (i & 1) * 112) * KB))
    STAMP(2, 5);
    const int fu = (7 * w + 6 * (lane & 1)) >> 3;
    pf = flag_ld(ws, FW_RT + (2 * 64 + rtA) * 32 + fu);
    F1_FIN(rtB, 1)
    STAMP(2, 6);
    VMBAR(0);
    if (tid == 0) flag_st(ws, FW_RT + (2 * 64 + rtB) * 32 + c7, tag);
    STAMP(2, 7);
#pragma unroll 1
    for (int ab = 0; ab < 2; ++ab) {
        const int rt = rtA + 2 * ab;
        poll_flags(ws, FW_RT + (2 * 64 + rt) * 32 + fu, tag, pf);
        STAMP(3, 0 + 4 * ab);
        u32x4 uf[14];
#pragma unroll
        for (int i = 0; i < 14; ++i) uf[i] = ldf(rs, l16, UF_OFF + (rt * 112 + 14 * w + ROT14(i)) * KB);
        pf = flag_ld(ws, FW_RT + (2 * 64 + rtB) * 32 + fu);
        SB();
        a0 = f32x16{}; a1 = f32x16{};
#pragma unroll
        for (int i = 0; i < 14; ++i) { a0 = MFMA(R[W2R(2 * i)], uf[i], a0); a1 = MFMA(R[W2R(2 * i + 1)], uf[i], a1); }
        STAMP(3, 1 + 4 * ab);
        if (ab) BAR();
        red_write(lds, w, lane, a0, a1);
        BAR();
        const f32x4 v = red_sum(lds, w, lane);
        const u32x2 hr = ab ? hres1 : hres0;
        *(u32x2*)(out + (size_t)(rt * 32 + r) * DM + c7 * 64 + (w >> 2) * 32 + (w & 3) * 8 + 4 * hi) =
            u32x2{pk2(v[0] + bflo(hr.x), v[1] + bfhi(hr.x)), pk2(v[2] + bflo(hr.y), v[3] + bfhi(hr.y))};
        STAMP(3, 2 + 4 * ab);
    }
    if (b == 0 && tid == 0) flag_st(ws, FW_GEN, tag);
    STAMP_FLUSH();
    asm volatile(".Lfb_end:" ::: "memory");
}

extern "C" void kernel_launch(void* const* d_in, const int* in_sizes, int n_in, void* d_out, int out_size, void* d_ws, size_t ws_size, hipStream_t stream) {
    (void)hipFuncSetAttribute((const void*)fused_block, hipFuncAttributeMaxDynamicSharedMemorySize, LDS_BYTES);
    fused_block<<<256, 512, LDS_BYTES, stream>>>((const unsigned short*)d_in[0], (const unsigned short*)d_in[1], (const unsigned short*)d_in[2], (const unsigned short*)d_in[4],
                                                 (const unsigned short*)d_in[5], (char*)d_ws, (unsigned short*)d_out);
}
```

```cpp
#include <hip/hip_runtime.h>
#include <hip/hip_bf16.h>
#include <stdint.h>

typedef short bf16x8 __attribute__((ext_vector_type(8)));
typedef float f32x16 __attribute__((ext_vector_type(16)));
typedef float f32x4 __attribute__((ext_vector_type(4)));
typedef float f32x2 __attribute__((ext_vector_type(2)));
typedef unsigned u32x4 __attribute__((ext_vector_type(4)));
typedef unsigned u32x2 __attribute__((ext_vector_type(2)));
typedef __bf16 bf16x2_t __attribute__((ext_vector_type(2)));
typedef short v4i16_t __attribute__((ext_vector_type(4)));
#define LAS __attribute__((address_space(3)))
#define GAS __attribute__((address_space(1)))
#define DI __device__ __forceinline__
#define MFMA(a, b, c) __builtin_amdgcn_mfma_f32_32x32x16_bf16(__builtin_bit_cast(bf16x8, (a)), __builtin_bit_cast(bf16x8, (b)), (c), 0, 0, 0)
#define BAR() asm volatile("s_waitcnt lgkmcnt(0)\n\ts_barrier" ::: "memory")
#define VMBAR(N) asm volatile("s_waitcnt vmcnt(" #N ") lgkmcnt(0)\n\ts_barrier" ::: "memory")
#define VMWAIT(N) asm volatile("s_waitcnt vmcnt(" #N ")" ::: "memory")
#define SB() __builtin_amdgcn_sched_barrier(0)
#define PIN(v) asm volatile("" : "+v"(v))
#define RLX_AGENT __ATOMIC_RELAXED, __HIP_MEMORY_SCOPE_AGENT

constexpr int SEQ = 2048, DM = 448, NH = 16, DH = 28, FF = 1792;
constexpr float EPS = 1e-5f;
constexpr float LOG2E = 1.4426950408889634f;
constexpr float QSCALE = 0.18898223650461362f * LOG2E;
constexpr unsigned ONES = 0x3f803f80u;
constexpr int KB = 1024;
constexpr int FW_GEN = 0, FW_KV = 64, FW_W = FW_KV + 16 * 32, FW_RT = FW_W + 256;
constexpr int KV_OFF = 32 * KB;
constexpr int OF_OFF = KV_OFF + 16 * 64 * 4 * KB;
constexpr int WO_OFF = OF_OFF + 64 * 32 * KB;
constexpr int HF_OFF = WO_OFF + 14 * 32 * KB;
constexpr int W1_OFF = HF_OFF + 64 * 28 * KB;
constexpr int UF_OFF = W1_OFF + 56 * 28 * KB;
constexpr int W2_OFF = UF_OFF + 64 * 112 * KB;
constexpr int WS_BYTES = W2_OFF + 14 * 112 * KB;
constexpr int SC1 = 16;
static_assert((FW_RT + 3 * 64 * 32) * 4 <= KV_OFF, "flag block");

DI unsigned pk2(float lo, float hi) { f32x2 v = {lo, hi}; return __builtin_bit_cast(unsigned, __builtin_convertvector(v, bf16x2_t)); }
DI float dot2(unsigned a, unsigned b, float c) { return __builtin_amdgcn_fdot2_f32_bf16(__builtin_bit_cast(bf16x2_t, a), __builtin_bit_cast(bf16x2_t, b), c, false); }
DI float sum8(const u32x4 f, float c) { return dot2(f.w, ONES, dot2(f.z, ONES, dot2(f.y, ONES, dot2(f.x, ONES, c)))); }
DI float sq8(const u32x4 f, float c) { return dot2(f.w, f.w, dot2(f.z, f.z, dot2(f.y, f.y, dot2(f.x, f.x, c)))); }
DI float bflo(unsigned u) { return __uint_as_float(u << 16); }
DI float bfhi(unsigned u) { return __uint_as_float(u & 0xffff0000u); }
DI float swap_add(float v) { auto r = __builtin_amdgcn_permlane32_swap(__float_as_uint(v), __float_as_uint(v), false, false); return __uint_as_float(r[0]) + __uint_as_float(r[1]); }
DI float swap_max(float v) { auto r = __builtin_amdgcn_permlane32_swap(__float_as_uint(v), __float_as_uint(v), false, false); return fmaxf(__uint_as_float(r[0]), __uint_as_float(r[1])); }
DI void glds16(const void* sbase, unsigned voff, unsigned lds_base) {
    asm volatile("s_mov_b32 m0, %2\n\ts_nop 0\n\tglobal_load_lds_dwordx4 %0, %1" :: "v"(voff), "s"(sbase), "s"(lds_base) : "memory");
}
DI void glds16c(const void* sbase, unsigned voff, unsigned lds_base) {
    asm volatile("s_mov_b32 m0, %2\n\ts_nop 0\n\tglobal_load_lds_dwordx4 %0, %1 sc1" :: "v"(voff), "s"(sbase), "s"(lds_base) : "memory");
}
DI u32x2 trd(LAS const char* p) { return __builtin_bit_cast(u32x2, __builtin_amdgcn_ds_read_tr16_b64_v4i16((LAS v4i16_t*)p)); }
DI u32x4 trfrag(LAS const char* p, int second) { u32x2 a = trd(p), b = trd(p + second); return u32x4{a.x, a.y, b.x, b.y}; }
DI u32x4 pack8(const f32x16& a, int s) {
    return u32x4{pk2(a[8 * s], a[8 * s + 1]), pk2(a[8 * s + 2], a[8 * s + 3]), pk2(a[8 * s + 4], a[8 * s + 5]), pk2(a[8 * s + 6], a[8 * s + 7])};
}
DI float max16(const f32x16& p) {
    float a = fmaxf(fmaxf(p[0], p[1]), p[2]), b = fmaxf(fmaxf(p[3], p[4]), p[5]);
    a = fmaxf(fmaxf(a, p[6]), p[7]); b = fmaxf(fmaxf(b, p[8]), p[9]);
    a = fmaxf(fmaxf(a, p[10]), p[11]); b = fmaxf(fmaxf(b, p[12]), p[13]);
    a = fmaxf(fmaxf(a, p[14]), p[15]);
    return fmaxf(a, b);
}
DI u32x2 split3(float m) {
    const float v = -m;
    const unsigned u1 = __float_as_uint(v) & 0xffff0000u; const float r1 = v - __uint_as_float(u1);
    const unsigned u2 = __float_as_uint(r1) & 0xffff0000u; const float r2 = r1 - __uint_as_float(u2);
    const unsigned u3 = __float_as_uint(r2) & 0xffff0000u;
    return u32x2{(u1 >> 16) | u2, u3 >> 16};
}
DI u32x4 corr_a(float cs, int hi) {
    const unsigned h = pk2(cs, cs); const float lo = cs - bflo(h); const unsigned l = pk2(lo, lo);
    return hi ? u32x4{0u, 0u, 0u, 0u} : u32x4{h, l, 0u, 0u};
}
DI u32x4 corr_b(float mu) {
    const unsigned h = pk2(-mu, 0.f); const unsigned d = pk2(-mu, -mu - bflo(h));
    return u32x4{d, d, 0u, 0u};
}
DI u32x4 ldf(__amdgpu_buffer_rsrc_t rs, int l16, int soff) { return __builtin_amdgcn_raw_buffer_load_b128(rs, l16, soff, SC1); }
DI void stf(u32x4 v, __amdgpu_buffer_rsrc_t rs, int l16, int soff) {
    __builtin_amdgcn_raw_buffer_store_b128(v, rs, l16, soff, SC1);
    asm volatile("s_nop 1" :: "v"(v) : "memory");
}
DI unsigned flag_ld(char* ws, int word) { return __hip_atomic_load((GAS unsigned*)ws + word, RLX_AGENT); }
DI void flag_st(char* ws, int word, unsigned v) { __hip_atomic_store((GAS unsigned*)ws + word, v, RLX_AGENT); }
DI void poll_flags(char* ws, int word, unsigned tag, unsigned first) {
    unsigned v = first;
    for (unsigned spins = 0; !__all(v == tag) && spins < (1u << 17); ++spins) v = flag_ld(ws, word);
    __builtin_amdgcn_fence(__ATOMIC_ACQUIRE, "wavefront");
}

#define STAMP(kid, i)
#define STAMP_FLUSH()
constexpr int STAMP_BYTES = 0;

constexpr int A_XW = 16 * 1040, A_SLOT = A_XW + 8 * KB, A_WQ = A_SLOT, A_PT = A_WQ + 28 * KB + 64, A_CS = 146 * KB;
static_assert(6 * A_SLOT <= A_CS && A_PT + 16 * KB <= 3 * A_SLOT, "phase 1 LDS");
constexpr int B_RED = 0;
constexpr int B_HS = 64 * KB;
constexpr int B_QS = 76 * KB;
constexpr int B_ST = 92 * KB;
constexpr int B_W1 = 96 * KB;
constexpr int B_FLAG = 152 * KB;
constexpr int STAMP_LDS = B_FLAG + 64;
constexpr int LDS_BYTES = STAMP_LDS + STAMP_BYTES;
static_assert(LDS_BYTES <= 160 * KB, "LDS");

DI void red_write(LAS char* lds, int w, int lane, const f32x16& a, const f32x16& c) {
    LAS f32x4* red = (LAS f32x4*)(lds + B_RED) + (w * 8) * 64 + lane;
#pragma unroll
    for (int g = 0; g < 4; ++g) {
        red[g * 64] = f32x4{a[4 * g], a[4 * g + 1], a[4 * g + 2], a[4 * g + 3]};
        red[(4 + g) * 64] = f32x4{c[4 * g], c[4 * g + 1], c[4 * g + 2], c[4 * g + 3]};
    }
}
DI f32x4 red_sum(LAS char* lds, int w, int lane) {
    LAS const f32x4* red = (LAS const f32x4*)(lds + B_RED) + w * 64 + lane;
    f32x4 v[8];
#pragma unroll
    for (int i = 0; i < 8; ++i) v[i] = red[i * 8 * 64];
    SB();
    return ((v[0] + v[1]) + (v[2] + v[3])) + ((v[4] + v[5]) + (v[6] + v[7]));
}

__global__ __launch_bounds__(512, 2) void fused_block(const unsigned short* __restrict__ x, const unsigned short* __restrict__ wqkv, const unsigned short* __restrict__ wo,
                                                      const unsigned short* __restrict__ w1, const unsigned short* __restrict__ w2, char* __restrict__ ws, unsigned short* __restrict__ out) {
    extern __shared__ __attribute__((aligned(16))) char smem[];
    const int tid = threadIdx.x, lane = tid & 63, w = __builtin_amdgcn_readfirstlane(tid >> 6), r = lane & 31, hi = lane >> 5;
    const int b = blockIdx.x, xcd = b & 7, bi = b >> 3;
    unsigned sink;
    {
        unsigned long long pc; unsigned len;
        asm volatile(".Lfb_begin:\n\ts_getpc_b64 %0\n\ts_mov_b32 %1, .Lfb_end-.Lfb_begin" : "=s"(pc), "=s"(len));
        const unsigned off = (unsigned)(((lane & 1) * 8 + w) * 32 + bi) * 128u;
        asm volatile("global_load_dword %0, %1, %2" : "=v"(sink) : "v"(off < len && len < 65536u ? off : 0u), "s"(pc) : "memory");
    }
    asm volatile("" :: "s"(x), "s"(wqkv), "s"(ws));
    LAS char* lds = (LAS char*)smem;
    const unsigned lds0 = (unsigned)(uintptr_t)smem;
    const int h = (xcd & 3) * 4 + (bi & 3), rb = (xcd >> 2) * 8 + (bi >> 2);
    const int rot = (2 * (bi & 3) + (bi >> 2)) % 7;
    const int l16 = lane * 16;
    const int trq = (lane & 15) >> 2, trc = ((lane >> 4) & 1) * 32 + (lane & 3) * 8;
    const __amdgpu_buffer_rsrc_t rs = __builtin_amdgcn_make_buffer_rsrc((void*)ws, 0, WS_BYTES, 0x00020000);
    LAS float* csl = (LAS float*)(lds + A_CS);
    volatile LAS unsigned* lflag = (volatile LAS unsigned*)(lds + B_FLAG);
    STAMP(0, 0);
    unsigned tag;
    u32x4 q0 = {}, q1 = {};
    if (__builtin_expect(w >= 4, 1)) {
        const int L = w - 4;
        const char* xb = (const char*)x + (size_t)(rb * 128 + 64 * (L >> 1) + 4 * (L & 1)) * 896;
        const unsigned xo0 = (lane >> 3) * (8 * 896) + (lane & 7) * 16, xo1 = xo0 + 896, xo2 = xo0 + 2 * 896, xo3 = xo0 + 3 * 896;
        const char* wkv = (const char*)wqkv + (size_t)(16 * L) * 2688 + 896 + 112 * h;
        const unsigned woff = (lane >> 3) * 2688 + (lane & 7) * 16, woff2 = woff + 8 * 2688;
#pragma unroll 1
        for (int s = 0; s < 7; ++s) {
            int kb = s + rot; kb = kb >= 7 ? kb - 7 : kb;
            const unsigned d_ = lds0 + (s == 6 ? 0 : s) * A_SLOT;
            if (s == 6) { while (lflag[4] < 4u) __builtin_amdgcn_s_sleep(1); }
            glds16(xb + kb * 128, xo0, d_ + L * 4160); glds16(xb + kb * 128, xo1, d_ + L * 4160 + 1040);
            glds16(xb + kb * 128, xo2, d_ + L * 4160 + 2080); glds16(xb + kb * 128, xo3, d_ + L * 4160 + 3120);
            if ((lane & 7) != 7) { glds16(wkv + (size_t)kb * (64 * 2688), woff, d_ + A_XW + L * 2048); glds16(wkv + (size_t)kb * (64 * 2688), woff2, d_ + A_XW + L * 2048 + 1024); }
            if (s == 0) BAR();
            else { VMWAIT(6); if (lane == 0) __hip_atomic_fetch_add((LAS unsigned*)&lflag[8 + s - 1], 1u, __ATOMIC_RELAXED, __HIP_MEMORY_SCOPE_WORKGROUP); if (s == 1) STAMP(0, 1); if (s == 4) STAMP(0, 2); }
        }
        VMWAIT(0);
        if (lane == 0) __hip_atomic_fetch_add((LAS unsigned*)&lflag[8 + 6], 1u, __ATOMIC_RELAXED, __HIP_MEMORY_SCOPE_WORKGROUP);
        STAMP(0, 3);
        PIN(sink);
        while (lflag[5] < 4u) __builtin_amdgcn_s_sleep(1);
        {
            const char* wq = (const char*)wqkv + (size_t)(16 * L) * 2688 + ((56 * h) & ~15);
            const unsigned qoff = (lane >> 2) * 2688 + (lane & 3) * 16;
#pragma unroll 1
            for (int i = 0; i < 7; ++i) { int kb = i + rot; kb = kb >= 7 ? kb - 7 : kb; glds16(wq + (size_t)kb * (64 * 2688), qoff, lds0 + A_WQ + (4 * i + L) * 1024); }
        }
        const char* psrc; int ppitch, pdst, pstep, k0;
        if (b < 112) { const int cp = b >> 2; k0 = (b & 3) * 7; psrc = (const char*)w1 + cp * 128; ppitch = FF * 2; pdst = W1_OFF + (cp * 2 * 28) * KB; pstep = 28 * KB; }
        else if (b < 224) { const int q = b - 112, cp = q >> 4; k0 = (q & 15) * 7; psrc = (const char*)w2 + cp * 128; ppitch = DM * 2; pdst = W2_OFF + (cp * 2 * 112) * KB; pstep = 112 * KB; }
        else { k0 = (b - 224) * 7; psrc = (const char*)wo; ppitch = DM * 2; pdst = WO_OFF; pstep = 32 * KB; }
        int pks[2], pd[2];
#pragma unroll
        for (int i = 0; i < 2; ++i) {
            const int kk = k0 + ((L + 4 * i < 7) ? L + 4 * i : L);
            const int cp = kk >> 5, ks = kk & 31;
            pks[i] = (b >= 224) ? (ks & 1) : 0; pd[i] = (b >= 224) ? pdst + (cp * 64 + ks) * KB : pdst + kk * KB;
#pragma unroll
            for (int u = 0; u < 2; ++u) {
                const int row = 8 * u + (lane >> 3), c = 16 * (ks & 1) + row;
                const int srow = (b >= 224) ? (ks >> 1) * DH + (c < DH ? c : DH - 1) : 16 * kk + row;
                glds16(psrc, (unsigned)(srow * ppitch + ((b >= 224) ? cp * 128 : 0) + (lane & 7) * 16), lds0 + A_PT + ((L * 2 + i) * 2 + u) * 1024);
            }
        }
        VMWAIT(4);
        float cq = 0.f;
        LAS const char* tq = lds + A_WQ + L * 1024 + (8 * hi + trq) * 64 + trc + ((56 * h) & 15);
#pragma unroll
        for (int i = 0; i < 7; ++i) cq = sum8(trfrag(tq + i * 4096, 256), cq);
        csl[(2 * 32 + r) * 8 + 2 * L + hi] = cq;
        STAMP(0, 4);
        BAR();
        tag = __builtin_amdgcn_readfirstlane(lflag[2]);
        LAS const char* tp = lds + A_PT + L * 4096 + (4 * hi + trq) * 128 + trc;
        VMWAIT(0);
#pragma unroll
        for (int i = 0; i < 2; ++i) {
            if (L + 4 * i < 7) {
#pragma unroll
                for (int ct = 0; ct < 2; ++ct) {
                    u32x4 f = trfrag(tp + i * 2048 + ct * 64, 1024);
                    if (pks[i] && hi) { f.z = 0u; f.w = 0u; }
                    stf(f, rs, l16, pd[i] + ct * pstep);
                }
            }
        }
        STAMP(0, 5);
    } else {
        const int rt = rb * 4 + w;
        const unsigned gen = flag_ld(ws, FW_GEN);
        if (tid < 16) lflag[tid] = 0u;
        if ((lane & 7) == 7) {
#pragma unroll
            for (int s = 0; s < 12; ++s) *(LAS u32x4*)(lds + (s >> 1) * A_SLOT + A_XW + w * 2048 + (s & 1) * 1024 + l16) = u32x4{ONES, ONES, ONES, ONES};
        }
        BAR();
        LAS const char* xr = lds + (8 * (w >> 1) + (r & 7)) * 1040 + (4 * (w & 1) + (r >> 3)) * 128 + hi * 16;
        LAS const char* tw = lds + A_XW + (8 * hi + trq) * 128 + trc;
        u32x4 xf[28];
        f32x16 a0 = {}, a1 = {};
        float s2 = 0.f, c0 = 0.f, c1 = 0.f;
        u32x4 wa[2][4], wb[2][4];
#define A_WAIT(s) { while (lflag[8 + (s)] < 4u) {} asm volatile("" ::: "memory"); }
#define A_RDX(s, i) { xf[4 * (s) + i] = *(LAS const u32x4*)(xr + ((s) % 6) * A_SLOT + (i ^ w) * 32); wa[(s) & 1][i] = trfrag(tw + ((s) % 6) * A_SLOT + (i ^ w) * 2048, 512); SB(); }
#define A_RDW(s, i) { wb[(s) & 1][i] = trfrag(tw + ((s) % 6) * A_SLOT + (i ^ w) * 2048 + 56, 512); SB(); }
#define A_STAGE(s, DONE) { if ((s) < 6) A_WAIT((s) + 1) \
        if ((DONE) && lane == 0) __hip_atomic_fetch_add((LAS unsigned*)&lflag[DONE], 1u, __ATOMIC_RELAXED, __HIP_MEMORY_SCOPE_WORKGROUP); \
        _Pragma("unroll") for (int i = 0; i < 4; ++i) { \
            a0 = MFMA(wa[(s) & 1][i], xf[4 * (s) + i], a0); PIN(a0); SB(); if ((s) < 6) A_RDX(((s) < 6 ? (s) + 1 : 6), i) \
            a1 = MFMA(wb[(s) & 1][i], xf[4 * (s) + i], a1); PIN(a1); SB(); if ((s) < 6) A_RDW(((s) < 6 ? (s) + 1 : 6), i) } \
        _Pragma("unroll") for (int i = 0; i < 4; ++i) s2 = sq8(xf[4 * (s) + i], s2); \
        c0 = sum8(wa[(s) & 1][0], c0); c1 = sum8(wb[(s) & 1][0], c1); PIN(s2); PIN(c0); PIN(c1); SB(); }
        A_WAIT(0)
#pragma unroll
        for (int i = 0; i < 4; ++i) { A_RDX(0, i) A_RDW(0, i) }
        A_STAGE(0, 4)
        STAMP(0, 1);
        A_STAGE(1, 0) A_STAGE(2, 5) A_STAGE(3, 0)
        STAMP(0, 2);
        A_STAGE(4, 0) A_STAGE(5, 0)
        csl[(0 * 32 + r) * 8 + 2 * w + hi] = c0; csl[(1 * 32 + r) * 8 + 2 * w + hi] = c1;
        asm volatile("s_waitcnt lgkmcnt(0)" ::: "memory");
        if (lane == 0) __hip_atomic_fetch_add((LAS unsigned*)&lflag[6], 1u, __ATOMIC_RELAXED, __HIP_MEMORY_SCOPE_WORKGROUP);
        c0 = 0.f; c1 = 0.f;
#pragma unroll
        for (int i = 0; i < 4; ++i) { s2 = sq8(xf[24 + i], s2); c0 = sum8(wa[0][i], c0); c1 = sum8(wb[0][i], c1); }
        PIN(s2); PIN(c0); PIN(c1); SB();
#pragma unroll
        for (int i = 0; i < 4; ++i) a1 = MFMA(wb[0][i], xf[24 + i], a1);
        PIN(a1); SB();
        while (lflag[6] < 4u) {}
        asm volatile("" ::: "memory");
#define A_CS(t) ({ const f32x4 p0 = *(LAS const f32x4*)(csl + ((t) * 32 + r) * 8), p1 = *(LAS const f32x4*)(csl + ((t) * 32 + r) * 8 + 4); ((p0.x + p0.y) + (p0.z + p0.w)) + ((p1.x + p1.y) + (p1.z + p1.w)); })
#pragma unroll
        for (int i = 0; i < 4; ++i) a0 = MFMA(wa[0][i], xf[24 + i], a0);
        STAMP(0, 3);
        tag = __builtin_amdgcn_readfirstlane(gen) + 1u;
        PIN(sink);
        if (tid == 0) lflag[2] = tag;
        s2 = swap_add(s2);
        const float mu = swap_add(hi ? a1[12] : 0.f) * (1.f / DM), var = fmaxf(s2 * (1.f / DM) - mu * mu, 0.f), rstd = __builtin_amdgcn_rsqf(var + EPS);
        const u32x4 cb = corr_b(mu);
        const int dst = KV_OFF + (((2 * h) & 15) * 64 + rt) * 4 * KB + (h < 8 ? 0 : 2 * KB);
        const float cs0 = A_CS(0) + swap_add(c0), cs1 = A_CS(1) + swap_add(c1);
        a1 = MFMA(corr_a(cs1, hi), cb, a1); a0 = MFMA(corr_a(cs0, hi), cb, a0);
        a0 *= rstd; a1 *= rstd;
        u32x4 f00 = pack8(a0, 0), f01 = pack8(a0, 1), f10 = pack8(a1, 0), f11 = pack8(a1, 1);
        const unsigned pz = h < 8 ? ONES : 0x00003f80u, pw = h < 8 ? 0x00003f80u : 0u;
        if (hi) { f01.z = pz; f01.w = pw; f11.z = pz; f11.w = pw; }
        if (h >= 8) {
            LAS char* tb = lds + 4 * A_SLOT + w * 4096;
            LAS char* wr = tb + r * 64 + hi * 8;
            *(LAS u32x2*)(wr) = u32x2{f00.x, f00.y}; *(LAS u32x2*)(wr + 16) = u32x2{f00.z, f00.w}; *(LAS u32x2*)(wr + 32) = u32x2{f01.x, f01.y}; *(LAS u32x2*)(wr + 48) = u32x2{f01.z, f01.w};
            *(LAS u32x2*)(wr + 2048) = u32x2{f10.x, f10.y}; *(LAS u32x2*)(wr + 2064) = u32x2{f10.z, f10.w}; *(LAS u32x2*)(wr + 2080) = u32x2{f11.x, f11.y}; *(LAS u32x2*)(wr + 2096) = u32x2{f11.z, f11.w};
            asm volatile("s_waitcnt lgkmcnt(0)" ::: "memory");
            LAS const char* rd = tb + (4 * hi + trq) * 64 + trc;
            f00 = trfrag(rd, 512); f01 = trfrag(rd + 1024, 512); f10 = trfrag(rd + 2048, 512); f11 = trfrag(rd + 3072, 512);
        }
        stf(f00, rs, l16, dst); stf(f01, rs, l16, dst + KB); stf(f10, rs, l16, dst + 64 * 4 * KB); stf(f11, rs, l16, dst + 64 * 4 * KB + KB);
        STAMP(0, 4);
        VMWAIT(0);
        if (lane == 0 && __hip_atomic_fetch_add((LAS unsigned*)&lflag[7], 1u, __ATOMIC_RELAXED, __HIP_MEMORY_SCOPE_WORKGROUP) == 3u) flag_st(ws, FW_KV + h * 32 + rb, tag);
        BAR();
        LAS const char* tq = lds + A_WQ + (8 * hi + trq) * 64 + trc + ((56 * h) & 15);
        f32x16 aq = {};
        u32x4 wq[8];
#pragma unroll
        for (int i = 0; i < 8; ++i) wq[i] = trfrag(tq + (i >> 2) * 4096 + ((i & 3) ^ w) * 1024, 256);
        SB();
#pragma unroll
        for (int i = 0; i < 28; ++i) {
            aq = MFMA(wq[i & 7], xf[i], aq); SB();
            if (i + 8 < 28) wq[i & 7] = trfrag(tq + ((i + 8) >> 2) * 4096 + ((i & 3) ^ w) * 1024, 256);
            SB();
        }
        aq = MFMA(corr_a(A_CS(2), hi), cb, aq);
        aq *= rstd * QSCALE;
        q0 = pack8(aq, 0); q1 = pack8(aq, 1);
        if (hi) { q1.z = 0u; q1.w = 0u; }
        STAMP(0, 5);
    }
    const bool post = bi < 28;
    const int gam = 4 * xcd + bi / 7, c7 = bi % 7;
    const int rtA = (gam >> 1) * 4 + (gam & 1);
    const __amdgpu_buffer_rsrc_t rw = __builtin_amdgcn_make_buffer_rsrc((void*)ws, 0, post ? WS_BYTES : 0, 0x00020000);
    u32x4 R[32];
    const int kvo = KV_OFF + (h * 64 + w * 8) * 4 * KB;
#define KVA(i) (kvo + ((((i) + 4 * rb) & 31) * KB))
    {
        const int fw = FW_KV + ((h >> 1) + 8 * (lane & 1)) * 32 + 2 * w + ((lane >> 1) & 1);
        poll_flags(ws, fw, tag, tag + 1u);
        STAMP(0, 6);
#pragma unroll
        for (int i = 0; i < 8; ++i) { R[i] = ldf(rs, l16, KVA(i)); SB(); }
        if (w < 4) {
            f32x16 sd = {};
            sd = MFMA(R[0], q0, sd); sd = MFMA(R[1], q1, sd);
            const float m = swap_max(max16(sd));
            if (hi) { const u32x2 p = split3(m); q1.z = p.x; q1.w = p.y; }
            *(LAS u32x4*)(lds + B_QS + w * 2048 + l16) = q0; *(LAS u32x4*)(lds + B_QS + w * 2048 + 1024 + l16) = q1;
        }
    }
    BAR();
    if (tid == 256) flag_st(ws, FW_W + b, tag);
    STAMP(0, 7);
    const int woo = WO_OFF + (2 * c7 * 32 + 4 * w) * KB, w1o = W1_OFF + (8 * c7 + w) * 28 * KB, w2o = W2_OFF + (2 * c7 * 112 + 14 * w) * KB;
#define NEXTW(i) ((i) < 4 ? woo + (i) * KB : (i) < 8 ? woo + (28 + (i)) * KB : w1o + ((i) - 8) * KB)
#define EXP4(S, i) { S[i] = __builtin_amdgcn_exp2f(S[i]); S[i + 1] = __builtin_amdgcn_exp2f(S[i + 1]); S[i + 2] = __builtin_amdgcn_exp2f(S[i + 2]); S[i + 3] = __builtin_amdgcn_exp2f(S[i + 3]); }
#define HOOK(PASS, i) if ((i) >= 32) { glds16c(ws + w1o + (16 + (i) - 32) * KB, l16, lds0 + B_W1 + (w * 7 + (i) - 32) * KB); } \
        else if ((i) >= 0) R[(i) & 31] = (PASS) ? ldf(rw, l16, NEXTW((i) & 31)) : ldf(rs, l16, KVA((i) & 31));
#define STEP(PASS, C, N, OC, OP, QN0, QN1, t, tn, tp, FIRST, LAST, L0, L1, L2, L3) { \
        if (!(LAST)) { N = MFMA(R[4 * (tn)], QN0, zero16); SB(); } \
        HOOK(PASS, L0) \
        EXP4(C, 0) p0.x = pk2(C[0], C[1]); p0.y = pk2(C[2], C[3]); PIN(p0); SB(); \
        if (!(FIRST)) { OP = MFMA(R[4 * (tp) + 3], p1, OP); SB(); } \
        HOOK(PASS, L1) \
        EXP4(C, 4) p0.z = pk2(C[4], C[5]); p0.w = pk2(C[6], C[7]); PIN(p0); SB(); \
        if (!(LAST)) { N = MFMA(R[4 * (tn) + 1], QN1, N); SB(); } \
        HOOK(PASS, L2) \
        EXP4(C, 8) p1.x = pk2(C[8], C[9]); p1.y = pk2(C[10], C[11]); PIN(p1); SB(); \
        OC = MFMA(R[4 * (t) + 2], p0, OC); SB(); \
        HOOK(PASS, L3) \
        EXP4(C, 12) p1.z = pk2(C[12], C[13]); p1.w = pk2(C[14], C[15]); PIN(p1); SB(); }
#define TILE(PASS, t, A0, A1, A2, A3, B0, B1, B2, B3) \
        STEP(PASS, sa, sb, oa, ob, qb0, qb1, t, t, (t) - 1 < 0 ? 0 : (t) - 1, (t) == 0, false, A0, A1, A2, A3) \
        STEP(PASS, sb, sa, ob, oa, qa0, qa1, t, (t) + 1 > 7 ? 7 : (t) + 1, t, false, (t) == 7, B0, B1, B2, B3)
#define TILE0(t) TILE(0, t, 4 * (t) + 8, -1, 4 * (t) + 9, -1, 4 * (t) + 10, -1, 4 * (t) + 11, -1)
#define COMBINE(PASS) { \
        LAS const float* lp = (LAS const float*)(lds + B_RED) + (((w >> 2) * 4 + 3) * 64 + 32 + r) * 4; \
        float lv[8]; _Pragma("unroll") for (int i = 0; i < 8; ++i) lv[i] = lp[i * 8 * 64 * 4]; \
        const f32x4 o = red_sum(lds, w, lane); \
        const float il = __builtin_amdgcn_rcpf(((lv[0] + lv[1]) + (lv[2] + lv[3])) + ((lv[4] + lv[5]) + (lv[6] + lv[7]))); \
        const int qt = rb * 4 + (PASS) * 2 + (w >> 2); \
        __builtin_amdgcn_raw_buffer_store_b64(u32x2{pk2(o[0] * il, o[1] * il), pk2(o[2] * il, o[3] * il)}, rs, l16 + (w & 1) * 8, OF_OFF + ((qt * 16 + h) * 2 + ((w >> 1) & 1)) * KB, SC1); }
#define FLAG_O(PASS) { if (tid < 2) flag_st(ws, FW_RT + (0 * 64 + rb * 4 + (PASS) * 2 + tid) * 32 + h, tag); }
#define LOADQ(PASS) \
        qa0 = *(LAS const u32x4*)(lds + B_QS + (PASS) * 4096 + l16); qa1 = *(LAS const u32x4*)(lds + B_QS + (PASS) * 4096 + 1024 + l16); \
        qb0 = *(LAS const u32x4*)(lds + B_QS + (PASS) * 4096 + 2048 + l16); qb1 = *(LAS const u32x4*)(lds + B_QS + (PASS) * 4096 + 3072 + l16);
#define PASS_BEGIN(PASS) LOADQ(PASS) oa = zero16; ob = zero16; sa = MFMA(R[0], qa0, zero16); sa = MFMA(R[1], qa1, sa); SB();
#define PASS_END(PASS) \
        ob = MFMA(R[31], p1, ob); \
        { const bool bad = hi && !(oa[12] < 1.0e18f && ob[12] < 1.0e18f); \
          if (__any(bad) && lane == 0) lflag[PASS] = 1u; } \
        red_write(lds, w, lane, oa, ob);
    u32x2 xres0, xres1;
    unsigned pfo;
    {
        const f32x16 zero16 = {};
        u32x4 qa0, qa1, qb0, qb1, p0, p1; f32x16 oa, ob, sa, sb;
        PASS_BEGIN(0)
        TILE0(0) TILE0(1) TILE0(2) TILE0(3) TILE0(4) TILE0(5)
        TILE(0, 6, -1, -1, -1, -1, -1, -1, -1, -1)
        const u32x4 wfl = __builtin_amdgcn_raw_buffer_load_b128(rs, l16, FW_W * 4, SC1);
        TILE(0, 7, -1, -1, -1, -1, -1, -1, -1, -1)
        STAMP(1, 0);
        PASS_END(0)
        {
            u32x4 v = wfl;
            for (unsigned spins = 0; !__all(v.x == tag && v.y == tag && v.z == tag && v.w == tag) && spins < (1u << 17); ++spins) {
                asm volatile("" ::: "memory");
                v = __builtin_amdgcn_raw_buffer_load_b128(rs, l16, FW_W * 4, SC1);
            }
            __builtin_amdgcn_fence(__ATOMIC_ACQUIRE, "wavefront");
        }
        PASS_BEGIN(1)
        TILE(1, 0, 0, 32, 1, 33, 34, 35, 36, 2)
        TILE(1, 1, 4, 3, 5, 37, 38, -1, -1, 6)
        BAR();
        STAMP(1, 1);
        const bool okA = lflag[0] == 0u;
        if (okA) COMBINE(0)
        TILE(1, 2, -1, -1, -1, -1, -1, -1, -1, -1)
        VMBAR(0);
        if (okA) FLAG_O(0)
        STAMP(1, 2);
        TILE(1, 3, 7, 8, 9, 10, 11, 12, 13, 14)
        TILE(1, 4, 16, 15, 17, -1, -1, -1, -1, 18)
        TILE(1, 5, 20, 19, 21, -1, -1, -1, -1, 22)
        {
            const unsigned short* xp = x + (size_t)((post ? rtA : 0) * 32 + r) * DM + c7 * 64 + (w >> 2) * 32 + (w & 3) * 8 + 4 * hi;
            xres0 = *(const u32x2*)xp; xres1 = *(const u32x2*)(xp + 64 * DM);
        }
        TILE(1, 6, -1, 23, -1, -1, -1, -1, -1, -1)
        pfo = flag_ld(ws, FW_RT + (0 * 64 + (post ? rtA : 0)) * 32 + 2 * w + (lane & 1));
        TILE(1, 7, -1, -1, -1, -1, -1, -1, -1, -1)
        STAMP(1, 3);
        PASS_END(1)
        BAR();
        const bool fb = lflag[0] != 0u || lflag[1] != 0u;
        if (__builtin_expect(fb, 0)) {
            if (lflag[1] == 0u) COMBINE(1)
#pragma unroll 1
            for (int p = 0; p < 2; ++p) {
                if (lflag[p] == 0u) continue;
                LOADQ(p)
                float ma = -INFINITY, mb = -INFINITY;
#pragma unroll 1
                for (int t = 0; t < 8; ++t) {
                    const u32x4 k0 = ldf(rs, l16, kvo + t * 4 * KB), k1 = ldf(rs, l16, kvo + t * 4 * KB + KB);
                    sa = MFMA(k0, qa0, zero16); sa = MFMA(k1, qa1, sa); sb = MFMA(k0, qb0, zero16); sb = MFMA(k1, qb1, sb);
                    ma = fmaxf(ma, max16(sa)); mb = fmaxf(mb, max16(sb));
                }
                ma = swap_max(ma); mb = swap_max(mb);
                LAS float* mx = (LAS float*)(lds + B_ST);
                mx[w * 64 + lane] = hi ? mb : ma;
                BAR();
#pragma unroll
                for (int i = 0; i < 8; ++i) { ma = fmaxf(ma, mx[i * 64 + r]); mb = fmaxf(mb, mx[i * 64 + 32 + r]); }
                if (hi) {
                    const u32x2 pa = split3(ma - (bflo(qa1.z) + bfhi(qa1.z) + bflo(qa1.w))), pb = split3(mb - (bflo(qb1.z) + bfhi(qb1.z) + bflo(qb1.w)));
                    qa1.z = pa.x; qa1.w = pa.y; qb1.z = pb.x; qb1.w = pb.y;
                }
                oa = zero16; ob = zero16;
#pragma unroll 1
                for (int t = 0; t < 8; ++t) {
                    const u32x4 k0 = ldf(rs, l16, kvo + t * 4 * KB), k1 = ldf(rs, l16, kvo + t * 4 * KB + KB), v0 = ldf(rs, l16, kvo + t * 4 * KB + 2 * KB), v1 = ldf(rs, l16, kvo + t * 4 * KB + 3 * KB);
                    sa = MFMA(k0, qa0, zero16); sa = MFMA(k1, qa1, sa); sb = MFMA(k0, qb0, zero16); sb = MFMA(k1, qb1, sb);
                    EXP4(sa, 0) EXP4(sa, 4) EXP4(sa, 8) EXP4(sa, 12) EXP4(sb, 0) EXP4(sb, 4) EXP4(sb, 8) EXP4(sb, 12)
                    oa = MFMA(v0, pack8(sa, 0), oa); oa = MFMA(v1, pack8(sa, 1), oa); ob = MFMA(v0, pack8(sb, 0), ob); ob = MFMA(v1, pack8(sb, 1), ob);
                }
                red_write(lds, w, lane, oa, ob);
                BAR();
                COMBINE(p)
                VMBAR(0);
                if (p == 0) FLAG_O(0)
            }
        }
        if (post) {
            poll_flags(ws, FW_RT + (0 * 64 + rtA) * 32 + 2 * w + (lane & 1), tag, pfo);
#pragma unroll
            for (int i = 0; i < 4; ++i) R[24 + i] = ldf(rs, l16, OF_OFF + (rtA * 32 + 4 * w + i) * KB);
        }
        if (!fb) COMBINE(1)
    }
    if (!post) { VMBAR(0); FLAG_O(1) STAMP_FLUSH(); return; }
    const int rtB = rtA + 2;
    const int ou = l16 + (w & 1) * 8;
    u32x4 E[5], of[4];
    f32x16 a0, a1;
#define WO_LOAD(rt) { _Pragma("unroll") for (int i = 0; i < 4; ++i) of[i] = ldf(rs, l16, OF_OFF + ((rt) * 32 + 4 * w + i) * KB); SB(); }
#define WO_MFMA() { a0 = f32x16{}; a1 = f32x16{}; _Pragma("unroll") for (int i = 0; i < 4; ++i) { a0 = MFMA(R[i], of[i], a0); a1 = MFMA(R[4 + i], of[i], a1); } }
#define WO_FIN(rt, xr, hr) { const f32x4 v = red_sum(lds, w, lane); \
        hr = u32x2{pk2(v[0] + bflo(xr.x), v[1] + bfhi(xr.x)), pk2(v[2] + bflo(xr.y), v[3] + bfhi(xr.y))}; \
        __builtin_amdgcn_raw_buffer_store_b64(hr, rs, ou, HF_OFF + ((rt) * 28 + c7 * 4 + (w >> 1)) * KB, SC1); }
#define ROT14(k) ((k) + 2 * c7 >= 14 ? (k) + 2 * c7 - 14 : (k) + 2 * c7)
#define W2R(i) ((i) < 8 ? 24 + (i) : (i) - 8)
#define W2_LOAD(lo, hi_) { _Pragma("unroll") for (int i = (lo); i < (hi_); ++i) R[W2R(i)] = ldf(rs, l16, w2o + (ROT14(i >> 1) + (i & 1) * 112) * KB); SB(); }
    u32x2 hres0, hres1;
#pragma unroll
    for (int i = 0; i < 4; ++i) of[i] = R[24 + i];
    WO_MFMA()
    VMBAR(0);
    FLAG_O(1)
    STAMP(1, 4);
#pragma unroll
    for (int i = 0; i < 5; ++i) E[i] = ldf(rs, l16, w1o + (23 + i) * KB);
    W2_LOAD(0, 4)
    red_write(lds, w, lane, a0, a1);
    BAR();
    WO_FIN(rtA, xres0, hres0)
    STAMP(1, 5);
    float cs1 = 0.f;
    LAS const char* w1l = lds + B_W1 + w * 7 * KB + l16;
#pragma unroll
    for (int i = 0; i < 16; ++i) cs1 = sum8(R[8 + i], cs1);
#pragma unroll
    for (int i = 0; i < 7; ++i) cs1 = sum8(*(LAS const u32x4*)(w1l + i * KB), cs1);
    PIN(cs1); SB();
    poll_flags(ws, FW_RT + (0 * 64 + rtB) * 32 + 2 * w + (lane & 1), tag, tag + 1u);
    WO_LOAD(rtB)
    BAR();
    if (tid == 0) flag_st(ws, FW_RT + (1 * 64 + rtA) * 32 + c7, tag);
    STAMP(1, 6);
    W2_LOAD(4, 8)
    WO_MFMA()
    red_write(lds, w, lane, a0, a1);
    BAR();
    WO_FIN(rtB, xres1, hres1)
    STAMP(1, 7);
#pragma unroll
    for (int i = 0; i < 5; ++i) cs1 = sum8(E[i], cs1);
    const u32x4 ca = corr_a(swap_add(cs1), hi);
    const int wp = w < 7 ? w : 6;
#define H_LOAD(rt) { _Pragma("unroll") for (int i = 0; i < 4; ++i) of[i] = ldf(rs, l16, HF_OFF + ((rt) * 28 + 4 * wp + i) * KB); SB(); }
#define H_STAGE(hs, ab) { float s1 = 0.f, s2 = 0.f; \
        _Pragma("unroll") for (int i = 0; i < 4; ++i) { *(LAS u32x4*)(lds + (hs) + l16 + (4 * wp + i) * KB) = of[i]; s1 = sum8(of[i], s1); s2 = sq8(of[i], s2); } \
        s1 = swap_add(s1); s2 = swap_add(s2); ((LAS f32x2*)(lds + B_ST))[(ab) * 224 + wp * 32 + r] = f32x2{s1, s2}; }
#define F1_MFMA(hs, HOOK) { u32x4 hb[4], wl[4]; a0 = f32x16{}; \
        _Pragma("unroll") for (int i = 0; i < 4; ++i) hb[i] = *(LAS const u32x4*)(lds + (hs) + l16 + i * KB); \
        SB(); \
        _Pragma("unroll") for (int i = 0; i < 28; ++i) { \
            a0 = MFMA(i < 16 ? R[8 + (i < 16 ? i : 0)] : i < 23 ? wl[i & 3] : E[i < 23 ? 0 : i - 23], hb[i & 3], a0); SB(); \
            if (i + 4 < 28) hb[i & 3] = *(LAS const u32x4*)(lds + (hs) + l16 + (i + 4) * KB); \
            if (i + 4 >= 16 && i + 4 < 23) wl[i & 3] = *(LAS const u32x4*)(w1l + (i + 4 - 16) * KB); \
            HOOK; SB(); } }
#define F1_FIN(rt, ab) { LAS const f32x2* st = (LAS const f32x2*)(lds + B_ST) + (ab) * 224 + r; f32x2 sv = st[0]; \
        _Pragma("unroll") for (int i = 1; i < 7; ++i) sv += st[i * 32]; \
        const float mu = sv.x * (1.f / DM), var = fmaxf(sv.y * (1.f / DM) - mu * mu, 0.f), rstd = __builtin_amdgcn_rsqf(var + EPS); \
        a0 = MFMA(ca, corr_b(mu), a0); \
        const float nr = -LOG2E * rstd; \
        _Pragma("unroll") for (int i = 0; i < 16; ++i) a0[i] = a0[i] * rstd * __builtin_amdgcn_rcpf(1.f + __builtin_amdgcn_exp2f(a0[i] * nr)); \
        const int ud = UF_OFF + ((rt) * 112 + (8 * c7 + w) * 2) * KB; \
        stf(pack8(a0, 0), rs, l16, ud); stf(pack8(a0, 1), rs, l16, ud + KB); }
    poll_flags(ws, FW_RT + (1 * 64 + rtA) * 32 + wp, tag, tag + 1u);
    H_LOAD(rtA)
    H_STAGE(B_HS, 0)
    BAR();
    if (tid == 0) flag_st(ws, FW_RT + (1 * 64 + rtB) * 32 + c7, tag);
    STAMP(2, 0);
    unsigned pf;
    F1_MFMA(B_HS, if (i < 8) R[i] = ldf(rs, l16, w2o + (ROT14((i + 8) >> 1) + (i & 1) * 112) * KB); if (i == 20) pf = flag_ld(ws, FW_RT + (1 * 64 + rtB) * 32 + wp))
    STAMP(2, 1);
    poll_flags(ws, FW_RT + (1 * 64 + rtB) * 32 + wp, tag, pf);
    H_LOAD(rtB)
    STAMP(2, 2);
    F1_FIN(rtA, 0)
    H_STAGE(B_RED, 1)
    STAMP(2, 3);
    VMBAR(0);
    if (tid == 0) flag_st(ws, FW_RT + (2 * 64 + rtA) * 32 + c7, tag);
    STAMP(2, 4);
    F1_MFMA(B_RED, if (i < 12) R[8 + (i < 12 ? i : 0)] = ldf(rs, l16, w2o + (ROT14(((i < 12 ? i : 0) + 16) >> 1) + (i & 1) * 112) * KB))
    STAMP(2, 5);
    const int fu = (7 * w + 6 * (lane & 1)) >> 3;
    pf = flag_ld(ws, FW_RT + (2 * 64 + rtA) * 32 + fu);
    F1_FIN(rtB, 1)
    STAMP(2, 6);
    VMBAR(0);
    if (tid == 0) flag_st(ws, FW_RT + (2 * 64 + rtB) * 32 + c7, tag);
    STAMP(2, 7);
    u32x4 uf[8];
    f32x16 b0, b1;
#define U_LD(j) uf[(j) & 7] = ldf(rs, l16, UF_OFF + ((rtA + 2 * ((j) / 14)) * 112 + 14 * w + ROT14((j) % 14)) * KB)
#define F2_FIN(red_, hr, o_) { const f32x4 v = red_sum(lds + (red_), w, lane); \
        o_ = u32x2{pk2(v[0] + bflo(hr.x), v[1] + bfhi(hr.x)), pk2(v[2] + bflo(hr.y), v[3] + bfhi(hr.y))}; }
    poll_flags(ws, FW_RT + (2 * 64 + rtA) * 32 + fu, tag, pf);
#pragma unroll
    for (int j = 0; j < 8; ++j) U_LD(j);
    SB();
    a0 = f32x16{}; a1 = f32x16{};
#pragma unroll
    for (int j = 0; j < 6; ++j) {
        a0 = MFMA(R[W2R(2 * j)], uf[j & 7], a0); a1 = MFMA(R[W2R(2 * j + 1)], uf[j & 7], a1); SB(); U_LD(j + 8);
        if (j == 3) pf = flag_ld(ws, FW_RT + (2 * 64 + rtB) * 32 + fu);
        SB();
    }
    poll_flags(ws, FW_RT + (2 * 64 + rtB) * 32 + fu, tag, pf);
#pragma unroll
    for (int j = 6; j < 14; ++j) { a0 = MFMA(R[W2R(2 * j)], uf[j & 7], a0); a1 = MFMA(R[W2R(2 * j + 1)], uf[j & 7], a1); SB(); U_LD(j + 8); SB(); }
    red_write(lds, w, lane, a0, a1);
    SB();
    b0 = f32x16{}; b1 = f32x16{};
#pragma unroll
    for (int j = 14; j < 28; ++j) { b0 = MFMA(R[W2R(2 * (j - 14))], uf[j & 7], b0); b1 = MFMA(R[W2R(2 * (j - 14) + 1)], uf[j & 7], b1); SB(); if (j + 8 < 28) U_LD(j + 8 < 28 ? j + 8 : 27); SB(); }
    red_write(lds + 64 * KB, w, lane, b0, b1);
    BAR();
    u32x2 oA, oB;
    F2_FIN(0, hres0, oA)
    F2_FIN(64 * KB, hres1, oB)
    unsigned short* op = out + (size_t)(rtA * 32 + r) * DM + c7 * 64 + (w >> 2) * 32 + (w & 3) * 8 + 4 * hi;
    *(u32x2*)op = oA; *(u32x2*)(op + 64 * DM) = oB;
    if (b == 0 && tid == 0) flag_st(ws, FW_GEN, tag);
    STAMP_FLUSH();
    asm volatile(".Lfb_end:" ::: "memory");
}

extern "C" void kernel_launch(void* const* d_in, const int* in_sizes, int n_in, void* d_out, int out_size, void* d_ws, size_t ws_size, hipStream_t stream) {
    (void)hipFuncSetAttribute((const void*)fused_block, hipFuncAttributeMaxDynamicSharedMemorySize, LDS_BYTES);
    fused_block<<<256, 512, LDS_BYTES, stream>>>((const unsigned short*)d_in[0], (const unsigned short*)d_in[1], (const unsigned short*)d_in[2], (const unsigned short*)d_in[4],
                                                 (const unsigned short*)d_in[5], (char*)d_ws, (unsigned short*)d_out);
}
```

```cpp
#include <hip/hip_runtime.h>
#include <hip/hip_bf16.h>
#include <stdint.h>

typedef short bf16x8 __attribute__((ext_vector_type(8)));
typedef float f32x16 __attribute__((ext_vector_type(16)));
typedef float f32x4 __attribute__((ext_vector_type(4)));
typedef float f32x2 __attribute__((ext_vector_type(2)));
typedef unsigned u32x4 __attribute__((ext_vector_type(4)));
typedef unsigned u32x2 __attribute__((ext_vector_type(2)));
typedef __bf16 bf16x2_t __attribute__((ext_vector_type(2)));
typedef short v4i16_t __attribute__((ext_vector_type(4)));
#define LAS __attribute__((address_space(3)))
#define GAS __attribute__((address_space(1)))
#define DI __device__ __forceinline__
#define MFMA(a, b, c) __builtin_amdgcn_mfma_f32_32x32x16_bf16(__builtin_bit_cast(bf16x8, (a)), __builtin_bit_cast(bf16x8, (b)), (c), 0, 0, 0)
#define BAR() asm volatile("s_waitcnt lgkmcnt(0)\n\ts_barrier" ::: "memory")
#define VMBAR(N) asm volatile("s_waitcnt vmcnt(" #N ") lgkmcnt(0)\n\ts_barrier" ::: "memory")
#define VMWAIT(N) asm volatile("s_waitcnt vmcnt(" #N ")" ::: "memory")
#define SB() __builtin_amdgcn_sched_barrier(0)
#define PIN(v) asm volatile("" : "+v"(v))
#define RLX_AGENT __ATOMIC_RELAXED, __HIP_MEMORY_SCOPE_AGENT

constexpr int SEQ = 2048, DM = 448, NH = 16, DH = 28, FF = 1792;
constexpr float EPS = 1e-5f;
constexpr float LOG2E = 1.4426950408889634f;
constexpr float QSCALE = 0.18898223650461362f * LOG2E;
constexpr unsigned ONES = 0x3f803f80u;
constexpr int KB = 1024;
constexpr int FW_GEN = 0, FW_KV = 64, FW_W = FW_KV + 16 * 32, FW_RT = FW_W + 256;
constexpr int KV_OFF = 32 * KB;
constexpr int OF_OFF = KV_OFF + 16 * 64 * 4 * KB;
constexpr int WO_OFF = OF_OFF + 64 * 32 * KB;
constexpr int HF_OFF = WO_OFF + 14 * 32 * KB;
constexpr int W1_OFF = HF_OFF + 64 * 28 * KB;
constexpr int UF_OFF = W1_OFF + 56 * 28 * KB;
constexpr int W2_OFF = UF_OFF + 64 * 112 * KB;
constexpr int WS_BYTES = W2_OFF + 14 * 112 * KB;
constexpr int SC1 = 16;
static_assert((FW_RT + 3 * 64 * 32) * 4 <= KV_OFF, "flag block");

DI unsigned pk2(float lo, float hi) { f32x2 v = {lo, hi}; return __builtin_bit_cast(unsigned, __builtin_convertvector(v, bf16x2_t)); }
DI float dot2(unsigned a, unsigned b, float c) { return __builtin_amdgcn_fdot2_f32_bf16(__builtin_bit_cast(bf16x2_t, a), __builtin_bit_cast(bf16x2_t, b), c, false); }
DI float sum8(const u32x4 f, float c) { return dot2(f.w, ONES, dot2(f.z, ONES, dot2(f.y, ONES, dot2(f.x, ONES, c)))); }
DI float sq8(const u32x4 f, float c) { return dot2(f.w, f.w, dot2(f.z, f.z, dot2(f.y, f.y, dot2(f.x, f.x, c)))); }
DI float bflo(unsigned u) { return __uint_as_float(u << 16); }
DI float bfhi(unsigned u) { return __uint_as_float(u & 0xffff0000u); }
DI float swap_add(float v) { auto r = __builtin_amdgcn_permlane32_swap(__float_as_uint(v), __float_as_uint(v), false, false); return __uint_as_float(r[0]) + __uint_as_float(r[1]); }
DI float swap_max(float v) { auto r = __builtin_amdgcn_permlane32_swap(__float_as_uint(v), __float_as_uint(v), false, false); return fmaxf(__uint_as_float(r[0]), __uint_as_float(r[1])); }
DI void glds16(const void* sbase, unsigned voff, unsigned lds_base) {
    asm volatile("s_mov_b32 m0, %2\n\ts_nop 0\n\tglobal_load_lds_dwordx4 %0, %1" :: "v"(voff), "s"(sbase), "s"(lds_base) : "memory");
}
DI void glds16c(const void* sbase, unsigned voff, unsigned lds_base) {
    asm volatile("s_mov_b32 m0, %2\n\ts_nop 0\n\tglobal_load_lds_dwordx4 %0, %1 sc1" :: "v"(voff), "s"(sbase), "s"(lds_base) : "memory");
}
DI u32x2 trd(LAS const char* p) { return __builtin_bit_cast(u32x2, __builtin_amdgcn_ds_read_tr16_b64_v4i16((LAS v4i16_t*)p)); }
DI u32x4 trfrag(LAS const char* p, int second) { u32x2 a = trd(p), b = trd(p + second); return u32x4{a.x, a.y, b.x, b.y}; }
DI u32x4 pack8(const f32x16& a, int s) {
    return u32x4{pk2(a[8 * s], a[8 * s + 1]), pk2(a[8 * s + 2], a[8 * s + 3]), pk2(a[8 * s + 4], a[8 * s + 5]), pk2(a[8 * s + 6], a[8 * s + 7])};
}
DI float max16(const f32x16& p) {
    float a = fmaxf(fmaxf(p[0], p[1]), p[2]), b = fmaxf(fmaxf(p[3], p[4]), p[5]);
    a = fmaxf(fmaxf(a, p[6]), p[7]); b = fmaxf(fmaxf(b, p[8]), p[9]);
    a = fmaxf(fmaxf(a, p[10]), p[11]); b = fmaxf(fmaxf(b, p[12]), p[13]);
    a = fmaxf(fmaxf(a, p[14]), p[15]);
    return fmaxf(a, b);
}
DI u32x2 split3(float m) {
    const float v = -m;
    const unsigned u1 = __float_as_uint(v) & 0xffff0000u; const float r1 = v - __uint_as_float(u1);
    const unsigned u2 = __float_as_uint(r1) & 0xffff0000u; const float r2 = r1 - __uint_as_float(u2);
    const unsigned u3 = __float_as_uint(r2) & 0xffff0000u;
    return u32x2{(u1 >> 16) | u2, u3 >> 16};
}
DI u32x4 corr_a(float cs, int hi) {
    const unsigned h = pk2(cs, cs); const float lo = cs - bflo(h); const unsigned l = pk2(lo, lo);
    return hi ? u32x4{0u, 0u, 0u, 0u} : u32x4{h, l, 0u, 0u};
}
DI u32x4 corr_b(float mu) {
    const unsigned h = pk2(-mu, 0.f); const unsigned d = pk2(-mu, -mu - bflo(h));
    return u32x4{d, d, 0u, 0u};
}
DI u32x4 ldf(__amdgpu_buffer_rsrc_t rs, int l16, int soff) { return __builtin_amdgcn_raw_buffer_load_b128(rs, l16, soff, SC1); }
DI void stf(u32x4 v, __amdgpu_buffer_rsrc_t rs, int l16, int soff) {
    __builtin_amdgcn_raw_buffer_store_b128(v, rs, l16, soff, SC1);
    asm volatile("s_nop 1" :: "v"(v) : "memory");
}
DI void sth(u32x2 p, __amdgpu_buffer_rsrc_t rs, int lane, int w, int soff) {
    const auto a = __builtin_amdgcn_permlane32_swap(p.x, p.x, false, false), c = __builtin_amdgcn_permlane32_swap(p.y, p.y, false, false);
    if (lane < 32) stf(u32x4{p.x, p.y, a[1], c[1]}, rs, lane * 16 + (w & 1) * 512, soff);
}
DI unsigned flag_ld(char* ws, int word) { return __hip_atomic_load((GAS unsigned*)ws + word, RLX_AGENT); }
DI void flag_st(char* ws, int word, unsigned v) { __hip_atomic_store((GAS unsigned*)ws + word, v, RLX_AGENT); }
DI void poll_flags(char* ws, int word, unsigned tag, unsigned first) {
    unsigned v = first;
    for (unsigned spins = 0; !__all(v == tag) && spins < (1u << 17); ++spins) v = flag_ld(ws, word);
    __builtin_amdgcn_fence(__ATOMIC_ACQUIRE, "wavefront");
}

#define STAMP(kid, i)
#define STAMP_FLUSH()
constexpr int STAMP_BYTES = 0;

constexpr int A_XW = 16 * 1040, A_SLOT = A_XW + 8 * KB, A_WQ = A_SLOT, A_PT = A_WQ + 28 * KB + 64, A_CS = 146 * KB;
static_assert(6 * A_SLOT <= A_CS && A_PT + 16 * KB <= 3 * A_SLOT, "phase 1 LDS");
constexpr int B_RED = 0;
constexpr int B_HS = 64 * KB;
constexpr int B_QS = 76 * KB;
constexpr int B_ST = 92 * KB;
constexpr int B_W1 = 96 * KB;
constexpr int B_FLAG = 152 * KB;
constexpr int STAMP_LDS = B_FLAG + 64;
constexpr int LDS_BYTES = STAMP_LDS + STAMP_BYTES;
static_assert(LDS_BYTES <= 160 * KB, "LDS");

DI void red_write(LAS char* lds, int w, int lane, const f32x16& a, const f32x16& c) {
    LAS f32x4* red = (LAS f32x4*)(lds + B_RED) + (w * 8) * 64 + lane;
#pragma unroll
    for (int g = 0; g < 4; ++g) {
        red[g * 64] = f32x4{a[4 * g], a[4 * g + 1], a[4 * g + 2], a[4 * g + 3]};
        red[(4 + g) * 64] = f32x4{c[4 * g], c[4 * g + 1], c[4 * g + 2], c[4 * g + 3]};
    }
}
DI f32x4 red_sum(LAS char* lds, int w, int lane) {
    LAS const f32x4* red = (LAS const f32x4*)(lds + B_RED) + ((w & 6) + (lane >> 5)) * 64 + 32 * (w & 1) + (lane & 31);
    f32x4 v[8];
#pragma unroll
    for (int i = 0; i < 8; ++i) v[i] = red[i * 8 * 64];
    SB();
    return ((v[0] + v[1]) + (v[2] + v[3])) + ((v[4] + v[5]) + (v[6] + v[7]));
}

__global__ __launch_bounds__(512, 2) void fused_block(const unsigned short* __restrict__ x, const unsigned short* __restrict__ wqkv, const unsigned short* __restrict__ wo,
                                                      const unsigned short* __restrict__ w1, const unsigned short* __restrict__ w2, char* __restrict__ ws, unsigned short* __restrict__ out) {
    extern __shared__ __attribute__((aligned(16))) char smem[];
    const int tid = threadIdx.x, lane = tid & 63, w = __builtin_amdgcn_readfirstlane(tid >> 6), r = lane & 31, hi = lane >> 5;
    const int b = blockIdx.x, xcd = b & 7, bi = b >> 3;
    unsigned sink;
    {
        unsigned long long pc; unsigned len;
        asm volatile(".Lfb_begin:\n\ts_getpc_b64 %0\n\ts_mov_b32 %1, .Lfb_end-.Lfb_begin" : "=s"(pc), "=s"(len));
        const unsigned off = (unsigned)(((lane & 1) * 8 + w) * 32 + bi) * 128u;
        asm volatile("global_load_dword %0, %1, %2" : "=v"(sink) : "v"(off < len && len < 65536u ? off : 0u), "s"(pc) : "memory");
    }
    asm volatile("" :: "s"(x), "s"(wqkv), "s"(ws));
    LAS char* lds = (LAS char*)smem;
    const unsigned lds0 = (unsigned)(uintptr_t)smem;
    const int h = (xcd & 3) * 4 + (bi & 3), rb = (xcd >> 2) * 8 + (bi >> 2);
    const int rot = (2 * (bi & 3) + (bi >> 2)) % 7;
    const int l16 = lane * 16;
    const int trq = (lane & 15) >> 2, trc = ((lane >> 4) & 1) * 32 + (lane & 3) * 8;
    const __amdgpu_buffer_rsrc_t rs = __builtin_amdgcn_make_buffer_rsrc((void*)ws, 0, WS_BYTES, 0x00020000);
    LAS float* csl = (LAS float*)(lds + A_CS);
    volatile LAS unsigned* lflag = (volatile LAS unsigned*)(lds + B_FLAG);
    STAMP(0, 0);
    unsigned tag;
    u32x4 q0 = {}, q1 = {};
    if (__builtin_expect(w >= 4, 1)) {
        const int L = w - 4;
        const char* xb = (const char*)x + (size_t)(rb * 128 + 64 * (L >> 1) + 4 * (L & 1)) * 896;
        const unsigned xo0 = (lane >> 3) * (8 * 896) + (lane & 7) * 16, xo1 = xo0 + 896, xo2 = xo0 + 2 * 896, xo3 = xo0 + 3 * 896;
        const char* wkv = (const char*)wqkv + (size_t)(16 * L) * 2688 + 896 + 112 * h;
        const unsigned woff = (lane >> 3) * 2688 + (lane & 7) * 16, woff2 = woff + 8 * 2688;
#pragma unroll 1
        for (int s = 0; s < 7; ++s) {
            int kb = s + rot; kb = kb >= 7 ? kb - 7 : kb;
            const unsigned d_ = lds0 + (s == 6 ? 0 : s) * A_SLOT;
            if (s == 6) { while (lflag[4] < 4u) __builtin_amdgcn_s_sleep(1); }
            glds16(xb + kb * 128, xo0, d_ + L * 4160); glds16(xb + kb * 128, xo1, d_ + L * 4160 + 1040);
            glds16(xb + kb * 128, xo2, d_ + L * 4160 + 2080); glds16(xb + kb * 128, xo3, d_ + L * 4160 + 3120);
            if ((lane & 7) != 7) { glds16(wkv + (size_t)kb * (64 * 2688), woff, d_ + A_XW + L * 2048); glds16(wkv + (size_t)kb * (64 * 2688), woff2, d_ + A_XW + L * 2048 + 1024); }
            if (s == 0) BAR();
            else { VMWAIT(6); if (lane == 0) __hip_atomic_fetch_add((LAS unsigned*)&lflag[8 + s - 1], 1u, __ATOMIC_RELAXED, __HIP_MEMORY_SCOPE_WORKGROUP); if (s == 1) STAMP(0, 1); if (s == 4) STAMP(0, 2); }
        }
        VMWAIT(0);
        if (lane == 0) __hip_atomic_fetch_add((LAS unsigned*)&lflag[8 + 6], 1u, __ATOMIC_RELAXED, __HIP_MEMORY_SCOPE_WORKGROUP);
        STAMP(0, 3);
        PIN(sink);
        while (lflag[5] < 4u) __builtin_amdgcn_s_sleep(1);
        {
            const char* wq = (const char*)wqkv + (size_t)(16 * L) * 2688 + ((56 * h) & ~15);
            const unsigned qoff = (lane >> 2) * 2688 + (lane & 3) * 16;
#pragma unroll 1
            for (int i = 0; i < 7; ++i) { int kb = i + rot; kb = kb >= 7 ? kb - 7 : kb; glds16(wq + (size_t)kb * (64 * 2688), qoff, lds0 + A_WQ + (4 * i + L) * 1024); }
        }
        const char* psrc; int ppitch, pdst, pstep, k0;
        if (b < 112) { const int cp = b >> 2; k0 = (b & 3) * 7; psrc = (const char*)w1 + cp * 128; ppitch = FF * 2; pdst = W1_OFF + (cp * 2 * 28) * KB; pstep = 28 * KB; }
        else if (b < 224) { const int q = b - 112, cp = q >> 4; k0 = (q & 15) * 7; psrc = (const char*)w2 + cp * 128; ppitch = DM * 2; pdst = W2_OFF + (cp * 2 * 112) * KB; pstep = 112 * KB; }
        else { k0 = (b - 224) * 7; psrc = (const char*)wo; ppitch = DM * 2; pdst = WO_OFF; pstep = 32 * KB; }
        int pks[2], pd[2];
#pragma unroll
        for (int i = 0; i < 2; ++i) {
            const int kk = k0 + ((L + 4 * i < 7) ? L + 4 * i : L);
            const int cp = kk >> 5, ks = kk & 31;
            pks[i] = (b >= 224) ? (ks & 1) : 0; pd[i] = (b >= 224) ? pdst + (cp * 64 + ks) * KB : pdst + kk * KB;
#pragma unroll
            for (int u = 0; u < 2; ++u) {
                const int row = 8 * u + (lane >> 3), c = 16 * (ks & 1) + row;
                const int srow = (b >= 224) ? (ks >> 1) * DH + (c < DH ? c : DH - 1) : 16 * kk + row;
                glds16(psrc, (unsigned)(srow * ppitch + ((b >= 224) ? cp * 128 : 0) + (lane & 7) * 16), lds0 + A_PT + ((L * 2 + i) * 2 + u) * 1024);
            }
        }
        VMWAIT(4);
        float cq = 0.f;
        LAS const char* tq = lds + A_WQ + L * 1024 + (8 * hi + trq) * 64 + trc + ((56 * h) & 15);
#pragma unroll
        for (int i = 0; i < 7; ++i) cq = sum8(trfrag(tq + i * 4096, 256), cq);
        csl[(2 * 32 + r) * 8 + 2 * L + hi] = cq;
        STAMP(0, 4);
        BAR();
        tag = __builtin_amdgcn_readfirstlane(lflag[2]);
        LAS const char* tp = lds + A_PT + L * 4096 + (4 * hi + trq) * 128 + trc;
        VMWAIT(0);
#pragma unroll
        for (int i = 0; i < 2; ++i) {
            if (L + 4 * i < 7) {
#pragma unroll
                for (int ct = 0; ct < 2; ++ct) {
                    u32x4 f = trfrag(tp + i * 2048 + ct * 64, 1024);
                    if (pks[i] && hi) { f.z = 0u; f.w = 0u; }
                    stf(f, rs, l16, pd[i] + ct * pstep);
                }
            }
        }
        STAMP(0, 5);
    } else {
        const int rt = rb * 4 + w;
        unsigned ftch = flag_ld(ws, ((b * 4 + w) & 255) * 32);
        const unsigned gen = flag_ld(ws, FW_GEN);
        if (tid < 16) lflag[tid] = 0u;
        if ((lane & 7) == 7) {
#pragma unroll
            for (int s = 0; s < 12; ++s) *(LAS u32x4*)(lds + (s >> 1) * A_SLOT + A_XW + w * 2048 + (s & 1) * 1024 + l16) = u32x4{ONES, ONES, ONES, ONES};
        }
        BAR();
        LAS const char* xr = lds + (8 * (w >> 1) + (r & 7)) * 1040 + (4 * (w & 1) + (r >> 3)) * 128 + hi * 16;
        LAS const char* tw = lds + A_XW + (8 * hi + trq) * 128 + trc;
        u32x4 xf[28];
        f32x16 a0 = {}, a1 = {};
        float s2 = 0.f, c0 = 0.f, c1 = 0.f;
        u32x4 wa[2][4], wb[2][4];
#define A_WAIT(s) { while (lflag[8 + (s)] < 4u) {} asm volatile("" ::: "memory"); }
#define A_RDX(s, i) { xf[4 * (s) + i] = *(LAS const u32x4*)(xr + ((s) % 6) * A_SLOT + (i ^ w) * 32); wa[(s) & 1][i] = trfrag(tw + ((s) % 6) * A_SLOT + (i ^ w) * 2048, 512); SB(); }
#define A_RDW(s, i) { wb[(s) & 1][i] = trfrag(tw + ((s) % 6) * A_SLOT + (i ^ w) * 2048 + 56, 512); SB(); }
#define A_STAGE(s, DONE) { if ((s) < 6) A_WAIT((s) + 1) \
        if ((DONE) && lane == 0) __hip_atomic_fetch_add((LAS unsigned*)&lflag[DONE], 1u, __ATOMIC_RELAXED, __HIP_MEMORY_SCOPE_WORKGROUP); \
        _Pragma("unroll") for (int i = 0; i < 4; ++i) { \
            a0 = MFMA(wa[(s) & 1][i], xf[4 * (s) + i], a0); PIN(a0); SB(); if ((s) < 6) A_RDX(((s) < 6 ? (s) + 1 : 6), i) \
            a1 = MFMA(wb[(s) & 1][i], xf[4 * (s) + i], a1); PIN(a1); SB(); if ((s) < 6) A_RDW(((s) < 6 ? (s) + 1 : 6), i) } \
        _Pragma("unroll") for (int i = 0; i < 4; ++i) s2 = sq8(xf[4 * (s) + i], s2); \
        c0 = sum8(wa[(s) & 1][0], c0); c1 = sum8(wb[(s) & 1][0], c1); PIN(s2); PIN(c0); PIN(c1); SB(); }
        A_WAIT(0)
#pragma unroll
        for (int i = 0; i < 4; ++i) { A_RDX(0, i) A_RDW(0, i) }
        A_STAGE(0, 4)
        STAMP(0, 1);
        A_STAGE(1, 0) A_STAGE(2, 5) A_STAGE(3, 0)
        STAMP(0, 2);
        A_STAGE(4, 0) A_STAGE(5, 0)
        csl[(0 * 32 + r) * 8 + 2 * w + hi] = c0; csl[(1 * 32 + r) * 8 + 2 * w + hi] = c1;
        asm volatile("s_waitcnt lgkmcnt(0)" ::: "memory");
        if (lane == 0) __hip_atomic_fetch_add((LAS unsigned*)&lflag[6], 1u, __ATOMIC_RELAXED, __HIP_MEMORY_SCOPE_WORKGROUP);
        c0 = 0.f; c1 = 0.f;
#pragma unroll
        for (int i = 0; i < 4; ++i) { s2 = sq8(xf[24 + i], s2); c0 = sum8(wa[0][i], c0); c1 = sum8(wb[0][i], c1); }
        PIN(s2); PIN(c0); PIN(c1); SB();
#pragma unroll
        for (int i = 0; i < 4; ++i) a1 = MFMA(wb[0][i], xf[24 + i], a1);
        PIN(a1); SB();
        while (lflag[6] < 4u) {}
        asm volatile("" ::: "memory");
#define A_CS(t) ({ const f32x4 p0 = *(LAS const f32x4*)(csl + ((t) * 32 + r) * 8), p1 = *(LAS const f32x4*)(csl + ((t) * 32 + r) * 8 + 4); ((p0.x + p0.y) + (p0.z + p0.w)) + ((p1.x + p1.y) + (p1.z + p1.w)); })
#pragma unroll
        for (int i = 0; i < 4; ++i) a0 = MFMA(wa[0][i], xf[24 + i], a0);
        STAMP(0, 3);
        tag = __builtin_amdgcn_readfirstlane(gen) + 1u;
        PIN(sink); PIN(ftch);
        if (tid == 0) lflag[2] = tag;
        s2 = swap_add(s2);
        const float mu = swap_add(hi ? a1[12] : 0.f) * (1.f / DM), var = fmaxf(s2 * (1.f / DM) - mu * mu, 0.f), rstd = __builtin_amdgcn_rsqf(var + EPS);
        const u32x4 cb = corr_b(mu);
        const int dst = KV_OFF + (((2 * h) & 15) * 64 + rt) * 4 * KB + (h < 8 ? 0 : 2 * KB);
        const float cs0 = A_CS(0) + swap_add(c0), cs1 = A_CS(1) + swap_add(c1);
        a1 = MFMA(corr_a(cs1, hi), cb, a1); a0 = MFMA(corr_a(cs0, hi), cb, a0);
        a0 *= rstd; a1 *= rstd;
        u32x4 f00 = pack8(a0, 0), f01 = pack8(a0, 1), f10 = pack8(a1, 0), f11 = pack8(a1, 1);
        const unsigned pz = h < 8 ? ONES : 0x00003f80u, pw = h < 8 ? 0x00003f80u : 0u;
        if (hi) { f01.z = pz; f01.w = pw; f11.z = pz; f11.w = pw; }
        if (h >= 8) {
            LAS char* tb = lds + 4 * A_SLOT + w * 4096;
            LAS char* wr = tb + r * 64 + hi * 8;
            *(LAS u32x2*)(wr) = u32x2{f00.x, f00.y}; *(LAS u32x2*)(wr + 16) = u32x2{f00.z, f00.w}; *(LAS u32x2*)(wr + 32) = u32x2{f01.x, f01.y}; *(LAS u32x2*)(wr + 48) = u32x2{f01.z, f01.w};
            *(LAS u32x2*)(wr + 2048) = u32x2{f10.x, f10.y}; *(LAS u32x2*)(wr + 2064) = u32x2{f10.z, f10.w}; *(LAS u32x2*)(wr + 2080) = u32x2{f11.x, f11.y}; *(LAS u32x2*)(wr + 2096) = u32x2{f11.z, f11.w};
            asm volatile("s_waitcnt lgkmcnt(0)" ::: "memory");
            LAS const char* rd = tb + (4 * hi + trq) * 64 + trc;
            f00 = trfrag(rd, 512); f01 = trfrag(rd + 1024, 512); f10 = trfrag(rd + 2048, 512); f11 = trfrag(rd + 3072, 512);
        }
        stf(f00, rs, l16, dst); stf(f01, rs, l16, dst + KB); stf(f10, rs, l16, dst + 64 * 4 * KB); stf(f11, rs, l16, dst + 64 * 4 * KB + KB);
        STAMP(0, 4);
        VMWAIT(0);
        if (lane == 0 && __hip_atomic_fetch_add((LAS unsigned*)&lflag[7], 1u, __ATOMIC_RELAXED, __HIP_MEMORY_SCOPE_WORKGROUP) == 3u) flag_st(ws, FW_KV + h * 32 + rb, tag);
        BAR();
        LAS const char* tq = lds + A_WQ + (8 * hi + trq) * 64 + trc + ((56 * h) & 15);
        f32x16 aq = {};
        u32x4 wq[8];
#pragma unroll
        for (int i = 0; i < 8; ++i) wq[i] = trfrag(tq + (i >> 2) * 4096 + ((i & 3) ^ w) * 1024, 256);
        SB();
#pragma unroll
        for (int i = 0; i < 28; ++i) {
            aq = MFMA(wq[i & 7], xf[i], aq); SB();
            if (i + 8 < 28) wq[i & 7] = trfrag(tq + ((i + 8) >> 2) * 4096 + ((i & 3) ^ w) * 1024, 256);
            SB();
        }
        aq = MFMA(corr_a(A_CS(2), hi), cb, aq);
        aq *= rstd * QSCALE;
        q0 = pack8(aq, 0); q1 = pack8(aq, 1);
        if (hi) { q1.z = 0u; q1.w = 0u; }
        STAMP(0, 5);
    }
    const bool post = bi < 28;
    const int gam = 4 * xcd + bi / 7, c7 = bi % 7;
    const int rtA = (gam >> 1) * 4 + (gam & 1);
    const __amdgpu_buffer_rsrc_t rw = __builtin_amdgcn_make_buffer_rsrc((void*)ws, 0, post ? WS_BYTES : 0, 0x00020000);
    u32x4 R[32];
    const int kvo = KV_OFF + (h * 64 + w * 8) * 4 * KB;
#define KVA(i) (kvo + ((((i) + 4 * rb) & 31) * KB))
    {
        const int fw = FW_KV + ((h >> 1) + 8 * (lane & 1)) * 32 + 2 * w + ((lane >> 1) & 1);
        poll_flags(ws, fw, tag, tag + 1u);
        STAMP(0, 6);
#pragma unroll
        for (int i = 0; i < 8; ++i) { R[i] = ldf(rs, l16, KVA(i)); SB(); }
        if (w < 4) {
            f32x16 sd = {};
            sd = MFMA(R[0], q0, sd); sd = MFMA(R[1], q1, sd);
            const float m = swap_max(max16(sd));
            if (hi) { const u32x2 p = split3(m); q1.z = p.x; q1.w = p.y; }
            *(LAS u32x4*)(lds + B_QS + w * 2048 + l16) = q0; *(LAS u32x4*)(lds + B_QS + w * 2048 + 1024 + l16) = q1;
        }
    }
    BAR();
    if (tid == 256) flag_st(ws, FW_W + b, tag);
    STAMP(0, 7);
    const int woo = WO_OFF + (2 * c7 * 32 + 4 * w) * KB, w1o = W1_OFF + (8 * c7 + w) * 28 * KB, w2o = W2_OFF + (2 * c7 * 112 + 14 * w) * KB;
#define NEXTW(i) ((i) < 4 ? woo + (i) * KB : (i) < 8 ? woo + (28 + (i)) * KB : w1o + ((i) - 8) * KB)
#define EXP4(S, i) { S[i] = __builtin_amdgcn_exp2f(S[i]); S[i + 1] = __builtin_amdgcn_exp2f(S[i + 1]); S[i + 2] = __builtin_amdgcn_exp2f(S[i + 2]); S[i + 3] = __builtin_amdgcn_exp2f(S[i + 3]); }
#define HOOK(PASS, i) if ((i) >= 32) { glds16c(ws + w1o + (16 + (i) - 32) * KB, l16, lds0 + B_W1 + (w * 7 + (i) - 32) * KB); } \
        else if ((i) >= 0) R[(i) & 31] = (PASS) ? ldf(rw, l16, NEXTW((i) & 31)) : ldf(rs, l16, KVA((i) & 31));
#define STEP(PASS, C, N, OC, OP, QN0, QN1, t, tn, tp, FIRST, LAST, L0, L1, L2, L3) { \
        if (!(LAST)) { N = MFMA(R[4 * (tn)], QN0, zero16); SB(); } \
        HOOK(PASS, L0) \
        EXP4(C, 0) p0.x = pk2(C[0], C[1]); p0.y = pk2(C[2], C[3]); PIN(p0); SB(); \
        if (!(FIRST)) { OP = MFMA(R[4 * (tp) + 3], p1, OP); SB(); } \
        HOOK(PASS, L1) \
        EXP4(C, 4) p0.z = pk2(C[4], C[5]); p0.w = pk2(C[6], C[7]); PIN(p0); SB(); \
        if (!(LAST)) { N = MFMA(R[4 * (tn) + 1], QN1, N); SB(); } \
        HOOK(PASS, L2) \
        EXP4(C, 8) p1.x = pk2(C[8], C[9]); p1.y = pk2(C[10], C[11]); PIN(p1); SB(); \
        OC = MFMA(R[4 * (t) + 2], p0, OC); SB(); \
        HOOK(PASS, L3) \
        EXP4(C, 12) p1.z = pk2(C[12], C[13]); p1.w = pk2(C[14], C[15]); PIN(p1); SB(); }
#define TILE(PASS, t, A0, A1, A2, A3, B0, B1, B2, B3) \
        STEP(PASS, sa, sb, oa, ob, qb0, qb1, t, t, (t) - 1 < 0 ? 0 : (t) - 1, (t) == 0, false, A0, A1, A2, A3) \
        STEP(PASS, sb, sa, ob, oa, qa0, qa1, t, (t) + 1 > 7 ? 7 : (t) + 1, t, false, (t) == 7, B0, B1, B2, B3)
#define TILE0(t) TILE(0, t, 4 * (t) + 8, -1, 4 * (t) + 9, -1, 4 * (t) + 10, -1, 4 * (t) + 11, -1)
#define COMBINE(PASS) { \
        LAS const float* lp = (LAS const float*)(lds + B_RED) + (((w >> 2) * 4 + 3) * 64 + 32 + r) * 4; \
        float lv[8]; _Pragma("unroll") for (int i = 0; i < 8; ++i) lv[i] = lp[i * 8 * 64 * 4]; \
        const f32x4 o = red_sum(lds, w, lane); \
        const float il = __builtin_amdgcn_rcpf(((lv[0] + lv[1]) + (lv[2] + lv[3])) + ((lv[4] + lv[5]) + (lv[6] + lv[7]))); \
        const int qt = rb * 4 + (PASS) * 2 + (w >> 2); \
        sth(u32x2{pk2(o[0] * il, o[1] * il), pk2(o[2] * il, o[3] * il)}, rs, lane, w, OF_OFF + ((qt * 16 + h) * 2 + ((w >> 1) & 1)) * KB); }
#define FLAG_O(PASS) { if (tid < 2) flag_st(ws, FW_RT + (0 * 64 + rb * 4 + (PASS) * 2 + tid) * 32 + h, tag); }
#define LOADQ(PASS) \
        qa0 = *(LAS const u32x4*)(lds + B_QS + (PASS) * 4096 + l16); qa1 = *(LAS const u32x4*)(lds + B_QS + (PASS) * 4096 + 1024 + l16); \
        qb0 = *(LAS const u32x4*)(lds + B_QS + (PASS) * 4096 + 2048 + l16); qb1 = *(LAS const u32x4*)(lds + B_QS + (PASS) * 4096 + 3072 + l16);
#define PASS_BEGIN(PASS) LOADQ(PASS) oa = zero16; ob = zero16; sa = MFMA(R[0], qa0, zero16); sa = MFMA(R[1], qa1, sa); SB();
#define PASS_END(PASS) \
        ob = MFMA(R[31], p1, ob); \
        { const bool bad = hi && !(oa[12] < 1.0e18f && ob[12] < 1.0e18f); \
          if (__any(bad) && lane == 0) lflag[PASS] = 1u; } \
        red_write(lds, w, lane, oa, ob);
    u32x2 xres0, xres1;
    unsigned pfo;
    {
        const f32x16 zero16 = {};
        u32x4 qa0, qa1, qb0, qb1, p0, p1; f32x16 oa, ob, sa, sb;
        PASS_BEGIN(0)
        TILE0(0) TILE0(1) TILE0(2) TILE0(3) TILE0(4) TILE0(5)
        TILE(0, 6, -1, -1, -1, -1, -1, -1, -1, -1)
        const u32x4 wfl = __builtin_amdgcn_raw_buffer_load_b128(rs, l16, FW_W * 4, SC1);
        TILE(0, 7, -1, -1, -1, -1, -1, -1, -1, -1)
        STAMP(1, 0);
        PASS_END(0)
        {
            u32x4 v = wfl;
            for (unsigned spins = 0; !__all(v.x == tag && v.y == tag && v.z == tag && v.w == tag) && spins < (1u << 17); ++spins) {
                asm volatile("" ::: "memory");
                v = __builtin_amdgcn_raw_buffer_load_b128(rs, l16, FW_W * 4, SC1);
            }
            __builtin_amdgcn_fence(__ATOMIC_ACQUIRE, "wavefront");
        }
        PASS_BEGIN(1)
        TILE(1, 0, 0, 32, 1, 33, 34, 35, 36, 2)
        TILE(1, 1, 4, 3, 5, 37, 38, -1, -1, 6)
        BAR();
        STAMP(1, 1);
        const bool okA = lflag[0] == 0u;
        if (okA) COMBINE(0)
        TILE(1, 2, -1, -1, -1, -1, -1, -1, -1, -1)
        VMBAR(0);
        if (okA) FLAG_O(0)
        STAMP(1, 2);
        TILE(1, 3, 7, 8, 9, 10, 11, 12, 13, 14)
        TILE(1, 4, 16, 15, 17, -1, -1, -1, -1, 18)
        TILE(1, 5, 20, 19, 21, -1, -1, -1, -1, 22)
        {
            const unsigned short* xp = x + (size_t)((post ? rtA : 0) * 32 + r) * DM + c7 * 64 + (w >> 1) * 16 + 8 * hi + 4 * (w & 1);
            xres0 = *(const u32x2*)xp; xres1 = *(const u32x2*)(xp + 64 * DM);
        }
        TILE(1, 6, -1, 23, -1, -1, -1, -1, -1, -1)
        pfo = flag_ld(ws, FW_RT + (0 * 64 + (post ? rtA : 0)) * 32 + 2 * w + (lane & 1));
        TILE(1, 7, -1, -1, -1, -1, -1, -1, -1, -1)
        STAMP(1, 3);
        PASS_END(1)
        BAR();
        const bool fb = lflag[0] != 0u || lflag[1] != 0u;
        if (__builtin_expect(fb, 0)) {
            if (lflag[1] == 0u) COMBINE(1)
#pragma unroll 1
            for (int p = 0; p < 2; ++p) {
                if (lflag[p] == 0u) continue;
                LOADQ(p)
                float ma = -INFINITY, mb = -INFINITY;
#pragma unroll 1
                for (int t = 0; t < 8; ++t) {
                    const u32x4 k0 = ldf(rs, l16, kvo + t * 4 * KB), k1 = ldf(rs, l16, kvo + t * 4 * KB + KB);
                    sa = MFMA(k0, qa0, zero16); sa = MFMA(k1, qa1, sa); sb = MFMA(k0, qb0, zero16); sb = MFMA(k1, qb1, sb);
                    ma = fmaxf(ma, max16(sa)); mb = fmaxf(mb, max16(sb));
                }
                ma = swap_max(ma); mb = swap_max(mb);
                LAS float* mx = (LAS float*)(lds + B_ST);
                mx[w * 64 + lane] = hi ? mb : ma;
                BAR();
#pragma unroll
                for (int i = 0; i < 8; ++i) { ma = fmaxf(ma, mx[i * 64 + r]); mb = fmaxf(mb, mx[i * 64 + 32 + r]); }
                if (hi) {
                    const u32x2 pa = split3(ma - (bflo(qa1.z) + bfhi(qa1.z) + bflo(qa1.w))), pb = split3(mb - (bflo(qb1.z) + bfhi(qb1.z) + bflo(qb1.w)));
                    qa1.z = pa.x; qa1.w = pa.y; qb1.z = pb.x; qb1.w = pb.y;
                }
                oa = zero16; ob = zero16;
#pragma unroll 1
                for (int t = 0; t < 8; ++t) {
                    const u32x4 k0 = ldf(rs, l16, kvo + t * 4 * KB), k1 = ldf(rs, l16, kvo + t * 4 * KB + KB), v0 = ldf(rs, l16, kvo + t * 4 * KB + 2 * KB), v1 = ldf(rs, l16, kvo + t * 4 * KB + 3 * KB);
                    sa = MFMA(k0, qa0, zero16); sa = MFMA(k1, qa1, sa); sb = MFMA(k0, qb0, zero16); sb = MFMA(k1, qb1, sb);
                    EXP4(sa, 0) EXP4(sa, 4) EXP4(sa, 8) EXP4(sa, 12) EXP4(sb, 0) EXP4(sb, 4) EXP4(sb, 8) EXP4(sb, 12)
                    oa = MFMA(v0, pack8(sa, 0), oa); oa = MFMA(v1, pack8(sa, 1), oa); ob = MFMA(v0, pack8(sb, 0), ob); ob = MFMA(v1, pack8(sb, 1), ob);
                }
                red_write(lds, w, lane, oa, ob);
                BAR();
                COMBINE(p)
                VMBAR(0);
                if (p == 0) FLAG_O(0)
            }
        }
        if (post) {
            poll_flags(ws, FW_RT + (0 * 64 + rtA) * 32 + 2 * w + (lane & 1), tag, pfo);
#pragma unroll
            for (int i = 0; i < 4; ++i) R[24 + i] = ldf(rs, l16, OF_OFF + (rtA * 32 + 4 * w + i) * KB);
        }
        if (!fb) COMBINE(1)
    }
    if (!post) { VMBAR(0); FLAG_O(1) STAMP_FLUSH(); return; }
    const int rtB = rtA + 2;
    u32x4 E[5], of[4];
    f32x16 a0, a1;
#define WO_LOAD(rt) { _Pragma("unroll") for (int i = 0; i < 4; ++i) of[i] = ldf(rs, l16, OF_OFF + ((rt) * 32 + 4 * w + i) * KB); SB(); }
#define WO_MFMA() { a0 = f32x16{}; a1 = f32x16{}; _Pragma("unroll") for (int i = 0; i < 4; ++i) { a0 = MFMA(R[i], of[i], a0); a1 = MFMA(R[4 + i], of[i], a1); } }
#define WO_FIN(rt, xr, hr) { const f32x4 v = red_sum(lds, w, lane); \
        hr = u32x2{pk2(v[0] + bflo(xr.x), v[1] + bfhi(xr.x)), pk2(v[2] + bflo(xr.y), v[3] + bfhi(xr.y))}; \
        sth(hr, rs, lane, w, HF_OFF + ((rt) * 28 + c7 * 4 + (w >> 1)) * KB); }
#define ROT14(k) ((k) + 2 * c7 >= 14 ? (k) + 2 * c7 - 14 : (k) + 2 * c7)
#define W2R(i) ((i) < 8 ? 24 + (i) : (i) - 8)
#define W2_LOAD(lo, hi_) { _Pragma("unroll") for (int i = (lo); i < (hi_); ++i) R[W2R(i)] = ldf(rs, l16, w2o + (ROT14(i >> 1) + (i & 1) * 112) * KB); SB(); }
    u32x2 hres0, hres1;
#pragma unroll
    for (int i = 0; i < 4; ++i) of[i] = R[24 + i];
    WO_MFMA()
    VMBAR(0);
    FLAG_O(1)
    STAMP(1, 4);
#pragma unroll
    for (int i = 0; i < 5; ++i) E[i] = ldf(rs, l16, w1o + (23 + i) * KB);
    W2_LOAD(0, 4)
    red_write(lds, w, lane, a0, a1);
    BAR();
    WO_FIN(rtA, xres0, hres0)
    STAMP(1, 5);
    float cs1 = 0.f;
    LAS const char* w1l = lds + B_W1 + w * 7 * KB + l16;
#pragma unroll
    for (int i = 0; i < 16; ++i) cs1 = sum8(R[8 + i], cs1);
#pragma unroll
    for (int i = 0; i < 7; ++i) cs1 = sum8(*(LAS const u32x4*)(w1l + i * KB), cs1);
    PIN(cs1); SB();
    poll_flags(ws, FW_RT + (0 * 64 + rtB) * 32 + 2 * w + (lane & 1), tag, tag + 1u);
    WO_LOAD(rtB)
    BAR();
    if (tid == 0) flag_st(ws, FW_RT + (1 * 64 + rtA) * 32 + c7, tag);
    STAMP(1, 6);
    W2_LOAD(4, 8)
    WO_MFMA()
    red_write(lds, w, lane, a0, a1);
    BAR();
    WO_FIN(rtB, xres1, hres1)
    STAMP(1, 7);
#pragma unroll
    for (int i = 0; i < 5; ++i) cs1 = sum8(E[i], cs1);
    const u32x4 ca = corr_a(swap_add(cs1), hi);
    const int wp = w < 7 ? w : 6;
#define H_LOAD(rt) { _Pragma("unroll") for (int i = 0; i < 4; ++i) of[i] = ldf(rs, l16, HF_OFF + ((rt) * 28 + 4 * wp + i) * KB); SB(); }
#define H_STAGE(hs, ab) { float s1 = 0.f, s2 = 0.f; \
        _Pragma("unroll") for (int i = 0; i < 4; ++i) { *(LAS u32x4*)(lds + (hs) + l16 + (4 * wp + i) * KB) = of[i]; s1 = sum8(of[i], s1); s2 = sq8(of[i], s2); } \
        s1 = swap_add(s1); s2 = swap_add(s2); ((LAS f32x2*)(lds + B_ST))[(ab) * 224 + wp * 32 + r] = f32x2{s1, s2}; }
#define F1_MFMA(hs, HOOK) { u32x4 hb[4], wl[4]; a0 = f32x16{}; \
        _Pragma("unroll") for (int i = 0; i < 4; ++i) hb[i] = *(LAS const u32x4*)(lds + (hs) + l16 + i * KB); \
        SB(); \
        _Pragma("unroll") for (int i = 0; i < 28; ++i) { \
            a0 = MFMA(i < 16 ? R[8 + (i < 16 ? i : 0)] : i < 23 ? wl[i & 3] : E[i < 23 ? 0 : i - 23], hb[i & 3], a0); SB(); \
            if (i + 4 < 28) hb[i & 3] = *(LAS const u32x4*)(lds + (hs) + l16 + (i + 4) * KB); \
            if (i + 4 >= 16 && i + 4 < 23) wl[i & 3] = *(LAS const u32x4*)(w1l + (i + 4 - 16) * KB); \
            HOOK; SB(); } }
#define F1_FIN(rt, ab) { LAS const f32x2* st = (LAS const f32x2*)(lds + B_ST) + (ab) * 224 + r; f32x2 sv = st[0]; \
        _Pragma("unroll") for (int i = 1; i < 7; ++i) sv += st[i * 32]; \
        const float mu = sv.x * (1.f / DM), var = fmaxf(sv.y * (1.f / DM) - mu * mu, 0.f), rstd = __builtin_amdgcn_rsqf(var + EPS); \
        a0 = MFMA(ca, corr_b(mu), a0); \
        const float nr = -LOG2E * rstd; \
        _Pragma("unroll") for (int i = 0; i < 16; ++i) a0[i] = a0[i] * rstd * __builtin_amdgcn_rcpf(1.f + __builtin_amdgcn_exp2f(a0[i] * nr)); \
        const int ud = UF_OFF + ((rt) * 112 + (8 * c7 + w) * 2) * KB; \
        stf(pack8(a0, 0), rs, l16, ud); stf(pack8(a0, 1), rs, l16, ud + KB); }
    poll_flags(ws, FW_RT + (1 * 64 + rtA) * 32 + wp, tag, tag + 1u);
    H_LOAD(rtA)
    H_STAGE(B_HS, 0)
    BAR();
    if (tid == 0) flag_st(ws, FW_RT + (1 * 64 + rtB) * 32 + c7, tag);
    STAMP(2, 0);
    unsigned pf;
    F1_MFMA(B_HS, if (i < 8) R[i] = ldf(rs, l16, w2o + (ROT14((i + 8) >> 1) + (i & 1) * 112) * KB); if (i == 20) pf = flag_ld(ws, FW_RT + (1 * 64 + rtB) * 32 + wp))
    STAMP(2, 1);
    poll_flags(ws, FW_RT + (1 * 64 + rtB) * 32 + wp, tag, pf);
    H_LOAD(rtB)
    STAMP(2, 2);
    F1_FIN(rtA, 0)
    H_STAGE(B_RED, 1)
    STAMP(2, 3);
    VMBAR(0);
    if (tid == 0) flag_st(ws, FW_RT + (2 * 64 + rtA) * 32 + c7, tag);
    STAMP(2, 4);
    F1_MFMA(B_RED, if (i < 12) R[8 + (i < 12 ? i : 0)] = ldf(rs, l16, w2o + (ROT14(((i < 12 ? i : 0) + 16) >> 1) + (i & 1) * 112) * KB))
    STAMP(2, 5);
    const int fu = (7 * w + 6 * (lane & 1)) >> 3;
    pf = flag_ld(ws, FW_RT + (2 * 64 + rtA) * 32 + fu);
    F1_FIN(rtB, 1)
    STAMP(2, 6);
    VMBAR(0);
    if (tid == 0) flag_st(ws, FW_RT + (2 * 64 + rtB) * 32 + c7, tag);
    STAMP(2, 7);
    u32x4 uf[8];
    f32x16 b0, b1;
#define U_LD(j) uf[(j) & 7] = ldf(rs, l16, UF_OFF + ((rtA + 2 * ((j) / 14)) * 112 + 14 * w + ROT14((j) % 14)) * KB)
#define F2_FIN(red_, hr, o_) { const f32x4 v = red_sum(lds + (red_), w, lane); \
        o_ = u32x2{pk2(v[0] + bflo(hr.x), v[1] + bfhi(hr.x)), pk2(v[2] + bflo(hr.y), v[3] + bfhi(hr.y))}; }
    poll_flags(ws, FW_RT + (2 * 64 + rtA) * 32 + fu, tag, pf);
#pragma unroll
    for (int j = 0; j < 8; ++j) U_LD(j);
    SB();
    a0 = f32x16{}; a1 = f32x16{};
#pragma unroll
    for (int j = 0; j < 6; ++j) {
        a0 = MFMA(R[W2R(2 * j)], uf[j & 7], a0); a1 = MFMA(R[W2R(2 * j + 1)], uf[j & 7], a1); SB(); U_LD(j + 8);
        if (j == 3) pf = flag_ld(ws, FW_RT + (2 * 64 + rtB) * 32 + fu);
        SB();
    }
    poll_flags(ws, FW_RT + (2 * 64 + rtB) * 32 + fu, tag, pf);
#pragma unroll
    for (int j = 6; j < 14; ++j) { a0 = MFMA(R[W2R(2 * j)], uf[j & 7], a0); a1 = MFMA(R[W2R(2 * j + 1)], uf[j & 7], a1); SB(); U_LD(j + 8); SB(); }
    red_write(lds, w, lane, a0, a1);
    SB();
    b0 = f32x16{}; b1 = f32x16{};
#pragma unroll
    for (int j = 14; j < 28; ++j) { b0 = MFMA(R[W2R(2 * (j - 14))], uf[j & 7], b0); b1 = MFMA(R[W2R(2 * (j - 14) + 1)], uf[j & 7], b1); SB(); if (j + 8 < 28) U_LD(j + 8 < 28 ? j + 8 : 27); SB(); }
    red_write(lds + 64 * KB, w, lane, b0, b1);
    BAR();
    u32x2 oA, oB;
    F2_FIN(0, hres0, oA)
    F2_FIN(64 * KB, hres1, oB)
    unsigned short* op = out + (size_t)(rtA * 32 + r) * DM + c7 * 64 + (w >> 1) * 16 + 8 * hi + 4 * (w & 1);
    *(u32x2*)op = oA; *(u32x2*)(op + 64 * DM) = oB;
    if (b == 0 && tid == 0) flag_st(ws, FW_GEN, tag);
    STAMP_FLUSH();
    asm volatile(".Lfb_end:" ::: "memory");
}

extern "C" void kernel_launch(void* const* d_in, const int* in_sizes, int n_in, void* d_out, int out_size, void* d_ws, size_t ws_size, hipStream_t stream) {
    (void)hipFuncSetAttribute((const void*)fused_block, hipFuncAttributeMaxDynamicSharedMemorySize, LDS_BYTES);
    fused_block<<<256, 512, LDS_BYTES, stream>>>((const unsigned short*)d_in[0], (const unsigned short*)d_in[1], (const unsigned short*)d_in[2], (const unsigned short*)d_in[4],
                                                 (const unsigned short*)d_in[5], (char*)d_ws, (unsigned short*)d_out);
}
```

```cpp
#include <hip/hip_runtime.h>
#include <hip/hip_bf16.h>
#include <stdint.h>

typedef short bf16x8 __attribute__((ext_vector_type(8)));
typedef float f32x16 __attribute__((ext_vector_type(16)));
typedef float f32x4 __attribute__((ext_vector_type(4)));
typedef float f32x2 __attribute__((ext_vector_type(2)));
typedef unsigned u32x4 __attribute__((ext_vector_type(4)));
typedef unsigned u32x2 __attribute__((ext_vector_type(2)));
typedef __bf16 bf16x2_t __attribute__((ext_vector_type(2)));
typedef short v4i16_t __attribute__((ext_vector_type(4)));
#define LAS __attribute__((address_space(3)))
#define GAS __attribute__((address_space(1)))
#define DI __device__ __forceinline__
#define MFMA(a, b, c) __builtin_amdgcn_mfma_f32_32x32x16_bf16(__builtin_bit_cast(bf16x8, (a)), __builtin_bit_cast(bf16x8, (b)), (c), 0, 0, 0)
#define BAR() asm volatile("s_waitcnt lgkmcnt(0)\n\ts_barrier" ::: "memory")
#define VMBAR(N) asm volatile("s_waitcnt vmcnt(" #N ") lgkmcnt(0)\n\ts_barrier" ::: "memory")
#define VMWAIT(N) asm volatile("s_waitcnt vmcnt(" #N ")" ::: "memory")
#define SB() __builtin_amdgcn_sched_barrier(0)
#define PIN(v) asm volatile("" : "+v"(v))
#define RLX_AGENT __ATOMIC_RELAXED, __HIP_MEMORY_SCOPE_AGENT

constexpr int SEQ = 2048, DM = 448, NH = 16, DH = 28, FF = 1792;
constexpr float EPS = 1e-5f;
constexpr float LOG2E = 1.4426950408889634f;
constexpr float QSCALE = 0.18898223650461362f * LOG2E;
constexpr unsigned ONES = 0x3f803f80u;
constexpr int KB = 1024;
constexpr int FW_GEN = 0, FW_KV = 64, FW_W = FW_KV + 16 * 32, FW_RT = FW_W + 256;
constexpr int KV_OFF = 32 * KB;
constexpr int OF_OFF = KV_OFF + 16 * 64 * 4 * KB;
constexpr int WO_OFF = OF_OFF + 64 * 32 * KB;
constexpr int HF_OFF = WO_OFF + 14 * 32 * KB;
constexpr int W1_OFF = HF_OFF + 64 * 28 * KB;
constexpr int UF_OFF = W1_OFF + 56 * 28 * KB;
constexpr int W2_OFF = UF_OFF + 64 * 112 * KB;
constexpr int WS_BYTES = W2_OFF + 14 * 112 * KB;
constexpr int SC1 = 16;
static_assert((FW_RT + 3 * 64 * 32) * 4 <= KV_OFF, "flag block");

DI unsigned pk2(float lo, float hi) { f32x2 v = {lo, hi}; return __builtin_bit_cast(unsigned, __builtin_convertvector(v, bf16x2_t)); }
DI float dot2(unsigned a, unsigned b, float c) { return __builtin_amdgcn_fdot2_f32_bf16(__builtin_bit_cast(bf16x2_t, a), __builtin_bit_cast(bf16x2_t, b), c, false); }
DI float sum8(const u32x4 f, float c) { return dot2(f.w, ONES, dot2(f.z, ONES, dot2(f.y, ONES, dot2(f.x, ONES, c)))); }
DI float sq8(const u32x4 f, float c) { return dot2(f.w, f.w, dot2(f.z, f.z, dot2(f.y, f.y, dot2(f.x, f.x, c)))); }
DI float bflo(unsigned u) { return __uint_as_float(u << 16); }
DI float bfhi(unsigned u) { return __uint_as_float(u & 0xffff0000u); }
DI float swap_add(float v) { auto r = __builtin_amdgcn_permlane32_swap(__float_as_uint(v), __float_as_uint(v), false, false); return __uint_as_float(r[0]) + __uint_as_float(r[1]); }
DI float swap_max(float v) { auto r = __builtin_amdgcn_permlane32_swap(__float_as_uint(v), __float_as_uint(v), false, false); return fmaxf(__uint_as_float(r[0]), __uint_as_float(r[1])); }
DI void glds16(const void* sbase, unsigned voff, unsigned lds_base) {
    asm volatile("s_mov_b32 m0, %2\n\ts_nop 0\n\tglobal_load_lds_dwordx4 %0, %1" :: "v"(voff), "s"(sbase), "s"(lds_base) : "memory");
}
DI void glds16c(const void* sbase, unsigned voff, unsigned lds_base) {
    asm volatile("s_mov_b32 m0, %2\n\ts_nop 0\n\tglobal_load_lds_dwordx4 %0, %1 sc1" :: "v"(voff), "s"(sbase), "s"(lds_base) : "memory");
}
DI u32x2 trd(LAS const char* p) { return __builtin_bit_cast(u32x2, __builtin_amdgcn_ds_read_tr16_b64_v4i16((LAS v4i16_t*)p)); }
DI u32x4 trfrag(LAS const char* p, int second) { u32x2 a = trd(p), b = trd(p + second); return u32x4{a.x, a.y, b.x, b.y}; }
DI u32x4 pack8(const f32x16& a, int s) {
    return u32x4{pk2(a[8 * s], a[8 * s + 1]), pk2(a[8 * s + 2], a[8 * s + 3]), pk2(a[8 * s + 4], a[8 * s + 5]), pk2(a[8 * s + 6], a[8 * s + 7])};
}
DI float max16(const f32x16& p) {
    float a = fmaxf(fmaxf(p[0], p[1]), p[2]), b = fmaxf(fmaxf(p[3], p[4]), p[5]);
    a = fmaxf(fmaxf(a, p[6]), p[7]); b = fmaxf(fmaxf(b, p[8]), p[9]);
    a = fmaxf(fmaxf(a, p[10]), p[11]); b = fmaxf(fmaxf(b, p[12]), p[13]);
    a = fmaxf(fmaxf(a, p[14]), p[15]);
    return fmaxf(a, b);
}
DI u32x2 split3(float m) {
    const float v = -m;
    const unsigned u1 = __float_as_uint(v) & 0xffff0000u; const float r1 = v - __uint_as_float(u1);
    const unsigned u2 = __float_as_uint(r1) & 0xffff0000u; const float r2 = r1 - __uint_as_float(u2);
    const unsigned u3 = __float_as_uint(r2) & 0xffff0000u;
    return u32x2{(u1 >> 16) | u2, u3 >> 16};
}
DI u32x4 corr_a(float cs, int hi) {
    const unsigned h = pk2(cs, cs); const float lo = cs - bflo(h); const unsigned l = pk2(lo, lo);
    return hi ? u32x4{0u, 0u, 0u, 0u} : u32x4{h, l, 0u, 0u};
}
DI u32x4 corr_b(float mu) {
    const unsigned h = pk2(-mu, 0.f); const unsigned d = pk2(-mu, -mu - bflo(h));
    return u32x4{d, d, 0u, 0u};
}
DI u32x4 ldf(__amdgpu_buffer_rsrc_t rs, int l16, int soff) { return __builtin_amdgcn_raw_buffer_load_b128(rs, l16, soff, SC1); }
DI void stf(u32x4 v, __amdgpu_buffer_rsrc_t rs, int l16, int soff) {
    __builtin_amdgcn_raw_buffer_store_b128(v, rs, l16, soff, SC1);
    asm volatile("s_nop 1" :: "v"(v) : "memory");
}
DI void sth(u32x2 p, __amdgpu_buffer_rsrc_t rs, int lane, int w, int soff) {
    const auto a = __builtin_amdgcn_permlane32_swap(p.x, p.x, false, false), c = __builtin_amdgcn_permlane32_swap(p.y, p.y, false, false);
    if (lane < 32) stf(u32x4{p.x, p.y, a[1], c[1]}, rs, lane * 16 + (w & 1) * 512, soff);
}
DI unsigned flag_ld(char* ws, int word) { return __hip_atomic_load((GAS unsigned*)ws + word, RLX_AGENT); }
DI void flag_st(char* ws, int word, unsigned v) { __hip_atomic_store((GAS unsigned*)ws + word, v, RLX_AGENT); }
DI void poll_flags(char* ws, int word, unsigned tag, unsigned first) {
    unsigned v = first;
    for (unsigned spins = 0; !__all(v == tag) && spins < (1u << 17); ++spins) v = flag_ld(ws, word);
    __builtin_amdgcn_fence(__ATOMIC_ACQUIRE, "wavefront");
}

#define STAMP(kid, i)
#define STAMP_FLUSH()
constexpr int STAMP_BYTES = 0;

constexpr int A_XW = 16 * 1040, A_SLOT = A_XW + 8 * KB, A_WQ = A_SLOT, A_PT = A_WQ + 28 * KB + 64, A_CS = 146 * KB;
static_assert(6 * A_SLOT <= A_CS && A_PT + 16 * KB <= 3 * A_SLOT, "phase 1 LDS");
constexpr int B_RED = 0;
constexpr int B_HS = 64 * KB;
constexpr int B_QS = 76 * KB;
constexpr int B_ST = 92 * KB;
constexpr int B_W1 = 96 * KB;
constexpr int B_FLAG = 152 * KB;
constexpr int STAMP_LDS = B_FLAG + 64;
constexpr int LDS_BYTES = STAMP_LDS + STAMP_BYTES;
static_assert(LDS_BYTES <= 160 * KB, "LDS");

DI void red_write(LAS char* lds, int w, int lane, const f32x16& a, const f32x16& c) {
    LAS f32x4* red = (LAS f32x4*)(lds + B_RED) + (w * 8) * 64 + lane;
#pragma unroll
    for (int g = 0; g < 4; ++g) {
        red[g * 64] = f32x4{a[4 * g], a[4 * g + 1], a[4 * g + 2], a[4 * g + 3]};
        red[(4 + g) * 64] = f32x4{c[4 * g], c[4 * g + 1], c[4 * g + 2], c[4 * g + 3]};
    }
}
DI f32x4 red_sum(LAS char* lds, int w, int lane) {
    LAS const f32x4* red = (LAS const f32x4*)(lds + B_RED) + ((w & 6) + (lane >> 5)) * 64 + 32 * (w & 1) + (lane & 31);
    f32x4 v[8];
#pragma unroll
    for (int i = 0; i < 8; ++i) v[i] = red[i * 8 * 64];
    SB();
    return ((v[0] + v[1]) + (v[2] + v[3])) + ((v[4] + v[5]) + (v[6] + v[7]));
}

__global__ __launch_bounds__(512, 2) void fused_block(const unsigned short* __restrict__ x, const unsigned short* __restrict__ wqkv, const unsigned short* __restrict__ wo,
                                                      const unsigned short* __restrict__ w1, const unsigned short* __restrict__ w2, char* __restrict__ ws, unsigned short* __restrict__ out) {
    extern __shared__ __attribute__((aligned(16))) char smem[];
    const int tid = threadIdx.x, lane = tid & 63, w = __builtin_amdgcn_readfirstlane(tid >> 6), r = lane & 31, hi = lane >> 5;
    const int b = blockIdx.x, xcd = b & 7, bi = b >> 3;
    unsigned sink;
    {
        unsigned long long pc; unsigned len;
        asm volatile(".Lfb_begin:\n\ts_getpc_b64 %0\n\ts_mov_b32 %1, .Lfb_end-.Lfb_begin\n\ts_max_u32 %1, %1, .Lfb_end2-.Lfb_begin" : "=s"(pc), "=s"(len) :: "scc");
        const unsigned off = (unsigned)(((lane & 1) * 8 + w) * 32 + bi) * 128u;
        asm volatile("global_load_dword %0, %1, %2" : "=v"(sink) : "v"(off < len && len < 65536u ? off : 0u), "s"(pc) : "memory");
    }
    asm volatile("" :: "s"(x), "s"(wqkv), "s"(ws));
    LAS char* lds = (LAS char*)smem;
    const unsigned lds0 = (unsigned)(uintptr_t)smem;
    const int h = (xcd & 3) * 4 + (bi & 3), rb = (xcd >> 2) * 8 + (bi >> 2);
    const int rot = (2 * (bi & 3) + (bi >> 2)) % 7;
    const int l16 = lane * 16;
    const int trq = (lane & 15) >> 2, trc = ((lane >> 4) & 1) * 32 + (lane & 3) * 8;
    const __amdgpu_buffer_rsrc_t rs = __builtin_amdgcn_make_buffer_rsrc((void*)ws, 0, WS_BYTES, 0x00020000);
    LAS float* csl = (LAS float*)(lds + A_CS);
    volatile LAS unsigned* lflag = (volatile LAS unsigned*)(lds + B_FLAG);
    STAMP(0, 0);
    unsigned tag;
    u32x4 q0 = {}, q1 = {};
    if (__builtin_expect(w >= 4, 1)) {
        const int L = w - 4;
        const char* xb = (const char*)x + (size_t)(rb * 128 + 64 * (L >> 1) + 4 * (L & 1)) * 896;
        const unsigned xo0 = (lane >> 3) * (8 * 896) + (lane & 7) * 16, xo1 = xo0 + 896, xo2 = xo0 + 2 * 896, xo3 = xo0 + 3 * 896;
        const char* wkv = (const char*)wqkv + (size_t)(16 * L) * 2688 + 896 + 112 * h;
        const unsigned woff = (lane >> 3) * 2688 + (lane & 7) * 16, woff2 = woff + 8 * 2688;
#pragma unroll 1
        for (int s = 0; s < 7; ++s) {
            int kb = s + rot; kb = kb >= 7 ? kb - 7 : kb;
            const unsigned d_ = lds0 + (s == 6 ? 0 : s) * A_SLOT;
            if (s == 6) { while (lflag[4] < 4u) __builtin_amdgcn_s_sleep(1); }
            glds16(xb + kb * 128, xo0, d_ + L * 4160); glds16(xb + kb * 128, xo1, d_ + L * 4160 + 1040);
            glds16(xb + kb * 128, xo2, d_ + L * 4160 + 2080); glds16(xb + kb * 128, xo3, d_ + L * 4160 + 3120);
            if ((lane & 7) != 7) { glds16(wkv + (size_t)kb * (64 * 2688), woff, d_ + A_XW + L * 2048); glds16(wkv + (size_t)kb * (64 * 2688), woff2, d_ + A_XW + L * 2048 + 1024); }
            if (s == 0) BAR();
            else { VMWAIT(6); if (lane == 0) __hip_atomic_fetch_add((LAS unsigned*)&lflag[8 + s - 1], 1u, __ATOMIC_RELAXED, __HIP_MEMORY_SCOPE_WORKGROUP); if (s == 1) STAMP(0, 1); if (s == 4) STAMP(0, 2); }
        }
        VMWAIT(0);
        if (lane == 0) __hip_atomic_fetch_add((LAS unsigned*)&lflag[8 + 6], 1u, __ATOMIC_RELAXED, __HIP_MEMORY_SCOPE_WORKGROUP);
        STAMP(0, 3);
        PIN(sink);
        while (lflag[5] < 4u) __builtin_amdgcn_s_sleep(1);
        {
            const char* wq = (const char*)wqkv + (size_t)(16 * L) * 2688 + ((56 * h) & ~15);
            const unsigned qoff = (lane >> 2) * 2688 + (lane & 3) * 16;
#pragma unroll 1
            for (int i = 0; i < 7; ++i) { int kb = i + rot; kb = kb >= 7 ? kb - 7 : kb; glds16(wq + (size_t)kb * (64 * 2688), qoff, lds0 + A_WQ + (4 * i + L) * 1024); }
        }
        const char* psrc; int ppitch, pdst, pstep, k0;
        if (b < 112) { const int cp = b >> 2; k0 = (b & 3) * 7; psrc = (const char*)w1 + cp * 128; ppitch = FF * 2; pdst = W1_OFF + (cp * 2 * 28) * KB; pstep = 28 * KB; }
        else if (b < 224) { const int q = b - 112, cp = q >> 4; k0 = (q & 15) * 7; psrc = (const char*)w2 + cp * 128; ppitch = DM * 2; pdst = W2_OFF + (cp * 2 * 112) * KB; pstep = 112 * KB; }
        else { k0 = (b - 224) * 7; psrc = (const char*)wo; ppitch = DM * 2; pdst = WO_OFF; pstep = 32 * KB; }
        int pks[2], pd[2];
#pragma unroll
        for (int i = 0; i < 2; ++i) {
            const int kk = k0 + ((L + 4 * i < 7) ? L + 4 * i : L);
            const int cp = kk >> 5, ks = kk & 31;
            pks[i] = (b >= 224) ? (ks & 1) : 0; pd[i] = (b >= 224) ? pdst + (cp * 64 + ks) * KB : pdst + kk * KB;
#pragma unroll
            for (int u = 0; u < 2; ++u) {
                const int row = 8 * u + (lane >> 3), c = 16 * (ks & 1) + row;
                const int srow = (b >= 224) ? (ks >> 1) * DH + (c < DH ? c : DH - 1) : 16 * kk + row;
                glds16(psrc, (unsigned)(srow * ppitch + ((b >= 224) ? cp * 128 : 0) + (lane & 7) * 16), lds0 + A_PT + ((L * 2 + i) * 2 + u) * 1024);
            }
        }
        VMWAIT(4);
        float cq = 0.f;
        LAS const char* tq = lds + A_WQ + L * 1024 + (8 * hi + trq) * 64 + trc + ((56 * h) & 15);
#pragma unroll
        for (int i = 0; i < 7; ++i) cq = sum8(trfrag(tq + i * 4096, 256), cq);
        csl[(2 * 32 + r) * 8 + 2 * L + hi] = cq;
        STAMP(0, 4);
        BAR();
        tag = __builtin_amdgcn_readfirstlane(lflag[2]);
        LAS const char* tp = lds + A_PT + L * 4096 + (4 * hi + trq) * 128 + trc;
        VMWAIT(0);
#pragma unroll
        for (int i = 0; i < 2; ++i) {
            if (L + 4 * i < 7) {
#pragma unroll
                for (int ct = 0; ct < 2; ++ct) {
                    u32x4 f = trfrag(tp + i * 2048 + ct * 64, 1024);
                    if (pks[i] && hi) { f.z = 0u; f.w = 0u; }
                    stf(f, rs, l16, pd[i] + ct * pstep);
                }
            }
        }
        STAMP(0, 5);
    } else {
        const int rt = rb * 4 + w;
        unsigned ftch = flag_ld(ws, ((b * 4 + w) & 255) * 32);
        const unsigned gen = flag_ld(ws, FW_GEN);
        if (tid < 16) lflag[tid] = 0u;
        if ((lane & 7) == 7) {
#pragma unroll
            for (int s = 0; s < 12; ++s) *(LAS u32x4*)(lds + (s >> 1) * A_SLOT + A_XW + w * 2048 + (s & 1) * 1024 + l16) = u32x4{ONES, ONES, ONES, ONES};
        }
        BAR();
        LAS const char* xr = lds + (8 * (w >> 1) + (r & 7)) * 1040 + (4 * (w & 1) + (r >> 3)) * 128 + hi * 16;
        LAS const char* tw = lds + A_XW + (8 * hi + trq) * 128 + trc;
        u32x4 xf[28];
        f32x16 a0 = {}, a1 = {};
        float s2 = 0.f, c0 = 0.f, c1 = 0.f;
        u32x4 wa[2][4], wb[2][4];
#define A_WAIT(s) { while (lflag[8 + (s)] < 4u) {} asm volatile("" ::: "memory"); }
#define A_RDX(s, i) { xf[4 * (s) + i] = *(LAS const u32x4*)(xr + ((s) % 6) * A_SLOT + (i ^ w) * 32); wa[(s) & 1][i] = trfrag(tw + ((s) % 6) * A_SLOT + (i ^ w) * 2048, 512); SB(); }
#define A_RDW(s, i) { wb[(s) & 1][i] = trfrag(tw + ((s) % 6) * A_SLOT + (i ^ w) * 2048 + 56, 512); SB(); }
#define A_STAGE(s, DONE) { if ((s) < 6) A_WAIT((s) + 1) \
        if ((DONE) && lane == 0) __hip_atomic_fetch_add((LAS unsigned*)&lflag[DONE], 1u, __ATOMIC_RELAXED, __HIP_MEMORY_SCOPE_WORKGROUP); \
        _Pragma("unroll") for (int i = 0; i < 4; ++i) { \
            a0 = MFMA(wa[(s) & 1][i], xf[4 * (s) + i], a0); PIN(a0); SB(); if ((s) < 6) A_RDX(((s) < 6 ? (s) + 1 : 6), i) \
            a1 = MFMA(wb[(s) & 1][i], xf[4 * (s) + i], a1); PIN(a1); SB(); if ((s) < 6) A_RDW(((s) < 6 ? (s) + 1 : 6), i) } \
        _Pragma("unroll") for (int i = 0; i < 4; ++i) s2 = sq8(xf[4 * (s) + i], s2); \
        c0 = sum8(wa[(s) & 1][0], c0); c1 = sum8(wb[(s) & 1][0], c1); PIN(s2); PIN(c0); PIN(c1); SB(); }
        A_WAIT(0)
#pragma unroll
        for (int i = 0; i < 4; ++i) { A_RDX(0, i) A_RDW(0, i) }
        A_STAGE(0, 4)
        STAMP(0, 1);
        A_STAGE(1, 0) A_STAGE(2, 5) A_STAGE(3, 0)
        STAMP(0, 2);
        A_STAGE(4, 0) A_STAGE(5, 0)
        csl[(0 * 32 + r) * 8 + 2 * w + hi] = c0; csl[(1 * 32 + r) * 8 + 2 * w + hi] = c1;
        asm volatile("s_waitcnt lgkmcnt(0)" ::: "memory");
        if (lane == 0) __hip_atomic_fetch_add((LAS unsigned*)&lflag[6], 1u, __ATOMIC_RELAXED, __HIP_MEMORY_SCOPE_WORKGROUP);
        c0 = 0.f; c1 = 0.f;
#pragma unroll
        for (int i = 0; i < 4; ++i) { s2 = sq8(xf[24 + i], s2); c0 = sum8(wa[0][i], c0); c1 = sum8(wb[0][i], c1); }
        PIN(s2); PIN(c0); PIN(c1); SB();
#pragma unroll
        for (int i = 0; i < 4; ++i) a1 = MFMA(wb[0][i], xf[24 + i], a1);
        PIN(a1); SB();
        while (lflag[6] < 4u) {}
        asm volatile("" ::: "memory");
#define A_CS(t) ({ const f32x4 p0 = *(LAS const f32x4*)(csl + ((t) * 32 + r) * 8), p1 = *(LAS const f32x4*)(csl + ((t) * 32 + r) * 8 + 4); ((p0.x + p0.y) + (p0.z + p0.w)) + ((p1.x + p1.y) + (p1.z + p1.w)); })
#pragma unroll
        for (int i = 0; i < 4; ++i) a0 = MFMA(wa[0][i], xf[24 + i], a0);
        STAMP(0, 3);
        tag = __builtin_amdgcn_readfirstlane(gen) + 1u;
        PIN(sink); PIN(ftch);
        if (tid == 0) lflag[2] = tag;
        s2 = swap_add(s2);
        const float mu = swap_add(hi ? a1[12] : 0.f) * (1.f / DM), var = fmaxf(s2 * (1.f / DM) - mu * mu, 0.f), rstd = __builtin_amdgcn_rsqf(var + EPS);
        const u32x4 cb = corr_b(mu);
        const int dst = KV_OFF + (((2 * h) & 15) * 64 + rt) * 4 * KB + (h < 8 ? 0 : 2 * KB);
        const float cs0 = A_CS(0) + swap_add(c0), cs1 = A_CS(1) + swap_add(c1);
        a1 = MFMA(corr_a(cs1, hi), cb, a1); a0 = MFMA(corr_a(cs0, hi), cb, a0);
        a0 *= rstd; a1 *= rstd;
        u32x4 f00 = pack8(a0, 0), f01 = pack8(a0, 1), f10 = pack8(a1, 0), f11 = pack8(a1, 1);
        const unsigned pz = h < 8 ? ONES : 0x00003f80u, pw = h < 8 ? 0x00003f80u : 0u;
        if (hi) { f01.z = pz; f01.w = pw; f11.z = pz; f11.w = pw; }
        if (h >= 8) {
            LAS char* tb = lds + 4 * A_SLOT + w * 4096;
            LAS char* wr = tb + r * 64 + hi * 8;
            *(LAS u32x2*)(wr) = u32x2{f00.x, f00.y}; *(LAS u32x2*)(wr + 16) = u32x2{f00.z, f00.w}; *(LAS u32x2*)(wr + 32) = u32x2{f01.x, f01.y}; *(LAS u32x2*)(wr + 48) = u32x2{f01.z, f01.w};
            *(LAS u32x2*)(wr + 2048) = u32x2{f10.x, f10.y}; *(LAS u32x2*)(wr + 2064) = u32x2{f10.z, f10.w}; *(LAS u32x2*)(wr + 2080) = u32x2{f11.x, f11.y}; *(LAS u32x2*)(wr + 2096) = u32x2{f11.z, f11.w};
            asm volatile("s_waitcnt lgkmcnt(0)" ::: "memory");
            LAS const char* rd = tb + (4 * hi + trq) * 64 + trc;
            f00 = trfrag(rd, 512); f01 = trfrag(rd + 1024, 512); f10 = trfrag(rd + 2048, 512); f11 = trfrag(rd + 3072, 512);
        }
        stf(f00, rs, l16, dst); stf(f01, rs, l16, dst + KB); stf(f10, rs, l16, dst + 64 * 4 * KB); stf(f11, rs, l16, dst + 64 * 4 * KB + KB);
        STAMP(0, 4);
        VMWAIT(0);
        if (lane == 0 && __hip_atomic_fetch_add((LAS unsigned*)&lflag[7], 1u, __ATOMIC_RELAXED, __HIP_MEMORY_SCOPE_WORKGROUP) == 3u) flag_st(ws, FW_KV + h * 32 + rb, tag);
        BAR();
        LAS const char* tq = lds + A_WQ + (8 * hi + trq) * 64 + trc + ((56 * h) & 15);
        f32x16 aq = {};
        u32x4 wq[8];
#pragma unroll
        for (int i = 0; i < 8; ++i) wq[i] = trfrag(tq + (i >> 2) * 4096 + ((i & 3) ^ w) * 1024, 256);
        SB();
#pragma unroll
        for (int i = 0; i < 28; ++i) {
            aq = MFMA(wq[i & 7], xf[i], aq); SB();
            if (i + 8 < 28) wq[i & 7] = trfrag(tq + ((i + 8) >> 2) * 4096 + ((i & 3) ^ w) * 1024, 256);
            SB();
        }
        aq = MFMA(corr_a(A_CS(2), hi), cb, aq);
        aq *= rstd * QSCALE;
        q0 = pack8(aq, 0); q1 = pack8(aq, 1);
        if (hi) { q1.z = 0u; q1.w = 0u; }
        PIN(q0); PIN(q1);
        asm volatile(".Lfb_end2:");
        STAMP(0, 5);
    }
    const bool post = bi < 28;
    const int gam = 4 * xcd + bi / 7, c7 = bi % 7;
    const int rtA = (gam >> 1) * 4 + (gam & 1);
    const __amdgpu_buffer_rsrc_t rw = __builtin_amdgcn_make_buffer_rsrc((void*)ws, 0, post ? WS_BYTES : 0, 0x00020000);
    u32x4 R[32];
    const int kvo = KV_OFF + (h * 64 + w * 8) * 4 * KB;
#define KVA(i) (kvo + ((((i) + 4 * rb) & 31) * KB))
    {
        const int fw = FW_KV + ((h >> 1) + 8 * (lane & 1)) * 32 + 2 * w + ((lane >> 1) & 1);
        poll_flags(ws, fw, tag, tag + 1u);
        STAMP(0, 6);
#pragma unroll
        for (int i = 0; i < 8; ++i) { R[i] = ldf(rs, l16, KVA(i)); SB(); }
        if (w < 4) {
            f32x16 sd = {};
            sd = MFMA(R[0], q0, sd); sd = MFMA(R[1], q1, sd);
            const float m = swap_max(max16(sd));
            if (hi) { const u32x2 p = split3(m); q1.z = p.x; q1.w = p.y; }
            *(LAS u32x4*)(lds + B_QS + w * 2048 + l16) = q0; *(LAS u32x4*)(lds + B_QS + w * 2048 + 1024 + l16) = q1;
        }
    }
    BAR();
    if (tid == 256) flag_st(ws, FW_W + b, tag);
    STAMP(0, 7);
    const int woo = WO_OFF + (2 * c7 * 32 + 4 * w) * KB, w1o = W1_OFF + (8 * c7 + w) * 28 * KB, w2o = W2_OFF + (2 * c7 * 112 + 14 * w) * KB;
#define NEXTW(i) ((i) < 4 ? woo + (i) * KB : (i) < 8 ? woo + (28 + (i)) * KB : w1o + ((i) - 8) * KB)
#define EXP4(S, i) { S[i] = __builtin_amdgcn_exp2f(S[i]); S[i + 1] = __builtin_amdgcn_exp2f(S[i + 1]); S[i + 2] = __builtin_amdgcn_exp2f(S[i + 2]); S[i + 3] = __builtin_amdgcn_exp2f(S[i + 3]); }
#define HOOK(PASS, i) if ((i) >= 32) { glds16c(ws + w1o + (16 + (i) - 32) * KB, l16, lds0 + B_W1 + (w * 7 + (i) - 32) * KB); } \
        else if ((i) >= 0) R[(i) & 31] = (PASS) ? ldf(rw, l16, NEXTW((i) & 31)) : ldf(rs, l16, KVA((i) & 31));
#define STEP(PASS, C, N, OC, OP, QN0, QN1, t, tn, tp, FIRST, LAST, L0, L1, L2, L3) { \
        if (!(LAST)) { N = MFMA(R[4 * (tn)], QN0, zero16); SB(); } \
        HOOK(PASS, L0) \
        EXP4(C, 0) p0.x = pk2(C[0], C[1]); p0.y = pk2(C[2], C[3]); PIN(p0); SB(); \
        if (!(FIRST)) { OP = MFMA(R[4 * (tp) + 3], p1, OP); SB(); } \
        HOOK(PASS, L1) \
        EXP4(C, 4) p0.z = pk2(C[4], C[5]); p0.w = pk2(C[6], C[7]); PIN(p0); SB(); \
        if (!(LAST)) { N = MFMA(R[4 * (tn) + 1], QN1, N); SB(); } \
        HOOK(PASS, L2) \
        EXP4(C, 8) p1.x = pk2(C[8], C[9]); p1.y = pk2(C[10], C[11]); PIN(p1); SB(); \
        OC = MFMA(R[4 * (t) + 2], p0, OC); SB(); \
        HOOK(PASS, L3) \
        EXP4(C, 12) p1.z = pk2(C[12], C[13]); p1.w = pk2(C[14], C[15]); PIN(p1); SB(); }
#define TILE(PASS, t, A0, A1, A2, A3, B0, B1, B2, B3) \
        STEP(PASS, sa, sb, oa, ob, qb0, qb1, t, t, (t) - 1 < 0 ? 0 : (t) - 1, (t) == 0, false, A0, A1, A2, A3) \
        STEP(PASS, sb, sa, ob, oa, qa0, qa1, t, (t) + 1 > 7 ? 7 : (t) + 1, t, false, (t) == 7, B0, B1, B2, B3)
#define TILE0(t) TILE(0, t, 4 * (t) + 8, -1, 4 * (t) + 9, -1, 4 * (t) + 10, -1, 4 * (t) + 11, -1)
#define COMBINE(PASS) { \
        LAS const float* lp = (LAS const float*)(lds + B_RED) + (((w >> 2) * 4 + 3) * 64 + 32 + r) * 4; \
        float lv[8]; _Pragma("unroll") for (int i = 0; i < 8; ++i) lv[i] = lp[i * 8 * 64 * 4]; \
        const f32x4 o = red_sum(lds, w, lane); \
        const float il = __builtin_amdgcn_rcpf(((lv[0] + lv[1]) + (lv[2] + lv[3])) + ((lv[4] + lv[5]) + (lv[6] + lv[7]))); \
        const int qt = rb * 4 + (PASS) * 2 + (w >> 2); \
        sth(u32x2{pk2(o[0] * il, o[1] * il), pk2(o[2] * il, o[3] * il)}, rs, lane, w, OF_OFF + ((qt * 16 + h) * 2 + ((w >> 1) & 1)) * KB); }
#define FLAG_O(PASS) { if (tid < 2) flag_st(ws, FW_RT + (0 * 64 + rb * 4 + (PASS) * 2 + tid) * 32 + h, tag); }
#define LOADQ(PASS) \
        qa0 = *(LAS const u32x4*)(lds + B_QS + (PASS) * 4096 + l16); qa1 = *(LAS const u32x4*)(lds + B_QS + (PASS) * 4096 + 1024 + l16); \
        qb0 = *(LAS const u32x4*)(lds + B_QS + (PASS) * 4096 + 2048 + l16); qb1 = *(LAS const u32x4*)(lds + B_QS + (PASS) * 4096 + 3072 + l16);
#define PASS_BEGIN(PASS) LOADQ(PASS) oa = zero16; ob = zero16; sa = MFMA(R[0], qa0, zero16); sa = MFMA(R[1], qa1, sa); SB();
#define PASS_END(PASS) \
        ob = MFMA(R[31], p1, ob); \
        { const bool bad = hi && !(oa[12] < 1.0e18f && ob[12] < 1.0e18f); \
          if (__any(bad) && lane == 0) lflag[PASS] = 1u; } \
        red_write(lds, w, lane, oa, ob);
    u32x2 xres0, xres1;
    unsigned pfo;
    {
        const f32x16 zero16 = {};
        u32x4 qa0, qa1, qb0, qb1, p0, p1; f32x16 oa, ob, sa, sb;
        PASS_BEGIN(0)
        TILE0(0) TILE0(1) TILE0(2) TILE0(3) TILE0(4) TILE0(5)
        TILE(0, 6, -1, -1, -1, -1, -1, -1, -1, -1)
        const u32x4 wfl = __builtin_amdgcn_raw_buffer_load_b128(rs, l16, FW_W * 4, SC1);
        TILE(0, 7, -1, -1, -1, -1, -1, -1, -1, -1)
        STAMP(1, 0);
        PASS_END(0)
        {
            u32x4 v = wfl;
            for (unsigned spins = 0; !__all(v.x == tag && v.y == tag && v.z == tag && v.w == tag) && spins < (1u << 17); ++spins) {
                asm volatile("" ::: "memory");
                v = __builtin_amdgcn_raw_buffer_load_b128(rs, l16, FW_W * 4, SC1);
            }
            __builtin_amdgcn_fence(__ATOMIC_ACQUIRE, "wavefront");
        }
        PASS_BEGIN(1)
        TILE(1, 0, 0, 32, 1, 33, 34, 35, 36, 2)
        TILE(1, 1, 4, 3, 5, 37, 38, -1, -1, 6)
        BAR();
        STAMP(1, 1);
        const bool okA = lflag[0] == 0u;
        if (okA) COMBINE(0)
        TILE(1, 2, -1, -1, -1, -1, -1, -1, -1, -1)
        VMBAR(0);
        if (okA) FLAG_O(0)
        STAMP(1, 2);
        TILE(1, 3, 7, 8, 9, 10, 11, 12, 13, 14)
        TILE(1, 4, 16, 15, 17, -1, -1, -1, -1, 18)
        TILE(1, 5, 20, 19, 21, -1, -1, -1, -1, 22)
        {
            const unsigned short* xp = x + (size_t)((post ? rtA : 0) * 32 + r) * DM + c7 * 64 + (w >> 1) * 16 + 8 * hi + 4 * (w & 1);
            xres0 = *(const u32x2*)xp; xres1 = *(const u32x2*)(xp + 64 * DM);
        }
        TILE(1, 6, -1, 23, -1, -1, -1, -1, -1, -1)
        pfo = flag_ld(ws, FW_RT + (0 * 64 + (post ? rtA : 0)) * 32 + 2 * w + (lane & 1));
        TILE(1, 7, -1, -1, -1, -1, -1, -1, -1, -1)
        STAMP(1, 3);
        PASS_END(1)
        BAR();
        const bool fb = lflag[0] != 0u || lflag[1] != 0u;
        if (__builtin_expect(fb, 0)) {
            if (lflag[1] == 0u) COMBINE(1)
#pragma unroll 1
            for (int p = 0; p < 2; ++p) {
                if (lflag[p] == 0u) continue;
                LOADQ(p)
                float ma = -INFINITY, mb = -INFINITY;
#pragma unroll 1
                for (int t = 0; t < 8; ++t) {
                    const u32x4 k0 = ldf(rs, l16, kvo + t * 4 * KB), k1 = ldf(rs, l16, kvo + t * 4 * KB + KB);
                    sa = MFMA(k0, qa0, zero16); sa = MFMA(k1, qa1, sa); sb = MFMA(k0, qb0, zero16); sb = MFMA(k1, qb1, sb);
                    ma = fmaxf(ma, max16(sa)); mb = fmaxf(mb, max16(sb));
                }
                ma = swap_max(ma); mb = swap_max(mb);
                LAS float* mx = (LAS float*)(lds + B_ST);
                mx[w * 64 + lane] = hi ? mb : ma;
                BAR();
#pragma unroll
                for (int i = 0; i < 8; ++i) { ma = fmaxf(ma, mx[i * 64 + r]); mb = fmaxf(mb, mx[i * 64 + 32 + r]); }
                if (hi) {
                    const u32x2 pa = split3(ma - (bflo(qa1.z) + bfhi(qa1.z) + bflo(qa1.w))), pb = split3(mb - (bflo(qb1.z) + bfhi(qb1.z) + bflo(qb1.w)));
                    qa1.z = pa.x; qa1.w = pa.y; qb1.z = pb.x; qb1.w = pb.y;
                }
                oa = zero16; ob = zero16;
#pragma unroll 1
                for (int t = 0; t < 8; ++t) {
                    const u32x4 k0 = ldf(rs, l16, kvo + t * 4 * KB), k1 = ldf(rs, l16, kvo + t * 4 * KB + KB), v0 = ldf(rs, l16, kvo + t * 4 * KB + 2 * KB), v1 = ldf(rs, l16, kvo + t * 4 * KB + 3 * KB);
                    sa = MFMA(k0, qa0, zero16); sa = MFMA(k1, qa1, sa); sb = MFMA(k0, qb0, zero16); sb = MFMA(k1, qb1, sb);
                    EXP4(sa, 0) EXP4(sa, 4) EXP4(sa, 8) EXP4(sa, 12) EXP4(sb, 0) EXP4(sb, 4) EXP4(sb, 8) EXP4(sb, 12)
                    oa = MFMA(v0, pack8(sa, 0), oa); oa = MFMA(v1, pack8(sa, 1), oa); ob = MFMA(v0, pack8(sb, 0), ob); ob = MFMA(v1, pack8(sb, 1), ob);
                }
                red_write(lds, w, lane, oa, ob);
                BAR();
                COMBINE(p)
                VMBAR(0);
                if (p == 0) FLAG_O(0)
            }
        }
        if (post) {
            poll_flags(ws, FW_RT + (0 * 64 + rtA) * 32 + 2 * w + (lane & 1), tag, pfo);
#pragma unroll
            for (int i = 0; i < 4; ++i) R[24 + i] = ldf(rs, l16, OF_OFF + (rtA * 32 + 4 * w + i) * KB);
        }
        if (!fb) COMBINE(1)
    }
    if (!post) { VMBAR(0); FLAG_O(1) STAMP_FLUSH(); return; }
    const int rtB = rtA + 2;
    u32x4 E[5], of[4];
    f32x16 a0, a1;
#define WO_LOAD(rt) { _Pragma("unroll") for (int i = 0; i < 4; ++i) of[i] = ldf(rs, l16, OF_OFF + ((rt) * 32 + 4 * w + i) * KB); SB(); }
#define WO_MFMA() { a0 = f32x16{}; a1 = f32x16{}; _Pragma("unroll") for (int i = 0; i < 4; ++i) { a0 = MFMA(R[i], of[i], a0); a1 = MFMA(R[4 + i], of[i], a1); } }
#define WO_FIN(rt, xr, hr) { const f32x4 v = red_sum(lds, w, lane); \
        hr = u32x2{pk2(v[0] + bflo(xr.x), v[1] + bfhi(xr.x)), pk2(v[2] + bflo(xr.y), v[3] + bfhi(xr.y))}; \
        sth(hr, rs, lane, w, HF_OFF + ((rt) * 28 + c7 * 4 + (w >> 1)) * KB); }
#define ROT14(k) ((k) + 2 * c7 >= 14 ? (k) + 2 * c7 - 14 : (k) + 2 * c7)
#define W2R(i) ((i) < 8 ? 24 + (i) : (i) - 8)
#define W2_LOAD(lo, hi_) { _Pragma("unroll") for (int i = (lo); i < (hi_); ++i) R[W2R(i)] = ldf(rs, l16, w2o + (ROT14(i >> 1) + (i & 1) * 112) * KB); SB(); }
    u32x2 hres0, hres1;
#pragma unroll
    for (int i = 0; i < 4; ++i) of[i] = R[24 + i];
    WO_MFMA()
    VMBAR(0);
    FLAG_O(1)
    STAMP(1, 4);
#pragma unroll
    for (int i = 0; i < 5; ++i) E[i] = ldf(rs, l16, w1o + (23 + i) * KB);
    W2_LOAD(0, 4)
    red_write(lds, w, lane, a0, a1);
    BAR();
    WO_FIN(rtA, xres0, hres0)
    STAMP(1, 5);
    float cs1 = 0.f;
    LAS const char* w1l = lds + B_W1 + w * 7 * KB + l16;
#pragma unroll
    for (int i = 0; i < 16; ++i) cs1 = sum8(R[8 + i], cs1);
#pragma unroll
    for (int i = 0; i < 7; ++i) cs1 = sum8(*(LAS const u32x4*)(w1l + i * KB), cs1);
    PIN(cs1); SB();
    poll_flags(ws, FW_RT + (0 * 64 + rtB) * 32 + 2 * w + (lane & 1), tag, tag + 1u);
    WO_LOAD(rtB)
    BAR();
    if (tid == 0) flag_st(ws, FW_RT + (1 * 64 + rtA) * 32 + c7, tag);
    STAMP(1, 6);
    W2_LOAD(4, 8)
    WO_MFMA()
    red_write(lds, w, lane, a0, a1);
    BAR();
    WO_FIN(rtB, xres1, hres1)
    STAMP(1, 7);
#pragma unroll
    for (int i = 0; i < 5; ++i) cs1 = sum8(E[i], cs1);
    const u32x4 ca = corr_a(swap_add(cs1), hi);
    const int wp = w < 7 ? w : 6;
#define H_LOAD(rt) { _Pragma("unroll") for (int i = 0; i < 4; ++i) of[i] = ldf(rs, l16, HF_OFF + ((rt) * 28 + 4 * wp + i) * KB); SB(); }
#define H_STAGE(hs, ab) { float s1 = 0.f, s2 = 0.f; \
        _Pragma("unroll") for (int i = 0; i < 4; ++i) { *(LAS u32x4*)(lds + (hs) + l16 + (4 * wp + i) * KB) = of[i]; s1 = sum8(of[i], s1); s2 = sq8(of[i], s2); } \
        s1 = swap_add(s1); s2 = swap_add(s2); ((LAS f32x2*)(lds + B_ST))[(ab) * 224 + wp * 32 + r] = f32x2{s1, s2}; }
#define F1_MFMA(hs, HOOK) { u32x4 hb[4], wl[4]; a0 = f32x16{}; \
        _Pragma("unroll") for (int i = 0; i < 4; ++i) hb[i] = *(LAS const u32x4*)(lds + (hs) + l16 + i * KB); \
        SB(); \
        _Pragma("unroll") for (int i = 0; i < 28; ++i) { \
            a0 = MFMA(i < 16 ? R[8 + (i < 16 ? i : 0)] : i < 23 ? wl[i & 3] : E[i < 23 ? 0 : i - 23], hb[i & 3], a0); SB(); \
            if (i + 4 < 28) hb[i & 3] = *(LAS const u32x4*)(lds + (hs) + l16 + (i + 4) * KB); \
            if (i + 4 >= 16 && i + 4 < 23) wl[i & 3] = *(LAS const u32x4*)(w1l + (i + 4 - 16) * KB); \
            HOOK; SB(); } }
#define F1_FIN(rt, ab) { LAS const f32x2* st = (LAS const f32x2*)(lds + B_ST) + (ab) * 224 + r; f32x2 sv = st[0]; \
        _Pragma("unroll") for (int i = 1; i < 7; ++i) sv += st[i * 32]; \
        const float mu = sv.x * (1.f / DM), var = fmaxf(sv.y * (1.f / DM) - mu * mu, 0.f), rstd = __builtin_amdgcn_rsqf(var + EPS); \
        a0 = MFMA(ca, corr_b(mu), a0); \
        const float nr = -LOG2E * rstd; \
        _Pragma("unroll") for (int i = 0; i < 16; ++i) a0[i] = a0[i] * rstd * __builtin_amdgcn_rcpf(1.f + __builtin_amdgcn_exp2f(a0[i] * nr)); \
        const int ud = UF_OFF + ((rt) * 112 + (8 * c7 + w) * 2) * KB; \
        stf(pack8(a0, 0), rs, l16, ud); stf(pack8(a0, 1), rs, l16, ud + KB); }
    poll_flags(ws, FW_RT + (1 * 64 + rtA) * 32 + wp, tag, tag + 1u);
    H_LOAD(rtA)
    H_STAGE(B_HS, 0)
    BAR();
    if (tid == 0) flag_st(ws, FW_RT + (1 * 64 + rtB) * 32 + c7, tag);
    STAMP(2, 0);
    unsigned pf;
    F1_MFMA(B_HS, if (i < 8) R[i] = ldf(rs, l16, w2o + (ROT14((i + 8) >> 1) + (i & 1) * 112) * KB); if (i == 20) pf = flag_ld(ws, FW_RT + (1 * 64 + rtB) * 32 + wp))
    STAMP(2, 1);
    poll_flags(ws, FW_RT + (1 * 64 + rtB) * 32 + wp, tag, pf);
    H_LOAD(rtB)
    STAMP(2, 2);
    F1_FIN(rtA, 0)
    H_STAGE(B_RED, 1)
    STAMP(2, 3);
    VMBAR(0);
    if (tid == 0) flag_st(ws, FW_RT + (2 * 64 + rtA) * 32 + c7, tag);
    STAMP(2, 4);
    F1_MFMA(B_RED, if (i < 12) R[8 + (i < 12 ? i : 0)] = ldf(rs, l16, w2o + (ROT14(((i < 12 ? i : 0) + 16) >> 1) + (i & 1) * 112) * KB))
    STAMP(2, 5);
    const int fu = (7 * w + 6 * (lane & 1)) >> 3;
    pf = flag_ld(ws, FW_RT + (2 * 64 + rtA) * 32 + fu);
    F1_FIN(rtB, 1)
    STAMP(2, 6);
    VMBAR(0);
    if (tid == 0) flag_st(ws, FW_RT + (2 * 64 + rtB) * 32 + c7, tag);
    STAMP(2, 7);
    u32x4 uf[8];
    f32x16 b0, b1;
#define U_LD(j) uf[(j) & 7] = ldf(rs, l16, UF_OFF + ((rtA + 2 * ((j) / 14)) * 112 + 14 * w + ROT14((j) % 14)) * KB)
#define F2_FIN(red_, hr, o_) { const f32x4 v = red_sum(lds + (red_), w, lane); \
        o_ = u32x2{pk2(v[0] + bflo(hr.x), v[1] + bfhi(hr.x)), pk2(v[2] + bflo(hr.y), v[3] + bfhi(hr.y))}; }
    poll_flags(ws, FW_RT + (2 * 64 + rtA) * 32 + fu, tag, pf);
#pragma unroll
    for (int j = 0; j < 8; ++j) U_LD(j);
    SB();
    a0 = f32x16{}; a1 = f32x16{};
#pragma unroll
    for (int j = 0; j < 6; ++j) {
        a0 = MFMA(R[W2R(2 * j)], uf[j & 7], a0); a1 = MFMA(R[W2R(2 * j + 1)], uf[j & 7], a1); SB(); U_LD(j + 8);
        if (j == 3) pf = flag_ld(ws, FW_RT + (2 * 64 + rtB) * 32 + fu);
        SB();
    }
    STAMP(3, 0);
    poll_flags(ws, FW_RT + (2 * 64 + rtB) * 32 + fu, tag, pf);
    STAMP(3, 1);
#pragma unroll
    for (int j = 6; j < 14; ++j) { a0 = MFMA(R[W2R(2 * j)], uf[j & 7], a0); a1 = MFMA(R[W2R(2 * j + 1)], uf[j & 7], a1); SB(); U_LD(j + 8); SB(); }
    red_write(lds, w, lane, a0, a1);
    SB();
    b0 = f32x16{}; b1 = f32x16{};
#pragma unroll
    for (int j = 14; j < 28; ++j) { b0 = MFMA(R[W2R(2 * (j - 14))], uf[j & 7], b0); b1 = MFMA(R[W2R(2 * (j - 14) + 1)], uf[j & 7], b1); SB(); if (j + 8 < 28) U_LD(j + 8 < 28 ? j + 8 : 27); SB(); }
    STAMP(3, 2);
    red_write(lds + 64 * KB, w, lane, b0, b1);
    BAR();
    u32x2 oA, oB;
    F2_FIN(0, hres0, oA)
    F2_FIN(64 * KB, hres1, oB)
    unsigned short* op = out + (size_t)(rtA * 32 + r) * DM + c7 * 64 + (w >> 1) * 16 + 8 * hi + 4 * (w & 1);
    *(u32x2*)op = oA; *(u32x2*)(op + 64 * DM) = oB;
    STAMP(3, 3);
    if (b == 0 && tid == 0) flag_st(ws, FW_GEN, tag);
    STAMP_FLUSH();
    asm volatile(".Lfb_end:" ::: "memory");
}

extern "C" void kernel_launch(void* const* d_in, const int* in_sizes, int n_in, void* d_out, int out_size, void* d_ws, size_t ws_size, hipStream_t stream) {
    (void)hipFuncSetAttribute((const void*)fused_block, hipFuncAttributeMaxDynamicSharedMemorySize, LDS_BYTES);
    fused_block<<<256, 512, LDS_BYTES, stream>>>((const unsigned short*)d_in[0], (const unsigned short*)d_in[1], (const unsigned short*)d_in[2], (const unsigned short*)d_in[4],
                                                 (const unsigned short*)d_in[5], (char*)d_ws, (unsigned short*)d_out);
}
```

```cpp
#include <hip/hip_runtime.h>
#include <hip/hip_bf16.h>
#include <stdint.h>

typedef short bf16x8 __attribute__((ext_vector_type(8)));
typedef float f32x16 __attribute__((ext_vector_type(16)));
typedef float f32x4 __attribute__((ext_vector_type(4)));
typedef float f32x2 __attribute__((ext_vector_type(2)));
typedef unsigned u32x4 __attribute__((ext_vector_type(4)));
typedef unsigned u32x2 __attribute__((ext_vector_type(2)));
typedef __bf16 bf16x2_t __attribute__((ext_vector_type(2)));
typedef short v4i16_t __attribute__((ext_vector_type(4)));
#define LAS __attribute__((address_space(3)))
#define GAS __attribute__((address_space(1)))
#define DI __device__ __forceinline__
#define MFMA(a, b, c) __builtin_amdgcn_mfma_f32_32x32x16_bf16(__builtin_bit_cast(bf16x8, (a)), __builtin_bit_cast(bf16x8, (b)), (c), 0, 0, 0)
#define BAR() asm volatile("s_waitcnt lgkmcnt(0)\n\ts_barrier" ::: "memory")
#define VMBAR(N) asm volatile("s_waitcnt vmcnt(" #N ") lgkmcnt(0)\n\ts_barrier" ::: "memory")
#define VMWAIT(N) asm volatile("s_waitcnt vmcnt(" #N ")" ::: "memory")
#define SB() __builtin_amdgcn_sched_barrier(0)
#define PIN(v) asm volatile("" : "+v"(v))
#define RLX_AGENT __ATOMIC_RELAXED, __HIP_MEMORY_SCOPE_AGENT

constexpr int SEQ = 2048, DM = 448, NH = 16, DH = 28, FF = 1792;
constexpr float EPS = 1e-5f;
constexpr float LOG2E = 1.4426950408889634f;
constexpr float QSCALE = 0.18898223650461362f * LOG2E;
constexpr unsigned ONES = 0x3f803f80u;
constexpr int KB = 1024;
constexpr int FW_GEN = 0, FW_KV = 64, FW_W = FW_KV + 16 * 32, FW_RT = FW_W + 256;
constexpr int KV_OFF = 32 * KB;
constexpr int OF_OFF = KV_OFF + 16 * 64 * 4 * KB;
constexpr int WO_OFF = OF_OFF + 64 * 32 * KB;
constexpr int HF_OFF = WO_OFF + 14 * 32 * KB;
constexpr int W1_OFF = HF_OFF + 64 * 28 * KB;
constexpr int UF_OFF = W1_OFF + 56 * 28 * KB;
constexpr int W2_OFF = UF_OFF + 64 * 112 * KB;
constexpr int WS_BYTES = W2_OFF + 14 * 112 * KB;
constexpr int SC1 = 16;
static_assert((FW_RT + 3 * 64 * 32) * 4 <= KV_OFF, "flag block");

DI unsigned pk2(float lo, float hi) { f32x2 v = {lo, hi}; return __builtin_bit_cast(unsigned, __builtin_convertvector(v, bf16x2_t)); }
DI float dot2(unsigned a, unsigned b, float c) { return __builtin_amdgcn_fdot2_f32_bf16(__builtin_bit_cast(bf16x2_t, a), __builtin_bit_cast(bf16x2_t, b), c, false); }
DI float sum8(const u32x4 f, float c) { return dot2(f.w, ONES, dot2(f.z, ONES, dot2(f.y, ONES, dot2(f.x, ONES, c)))); }
DI float sq8(const u32x4 f, float c) { return dot2(f.w, f.w, dot2(f.z, f.z, dot2(f.y, f.y, dot2(f.x, f.x, c)))); }
DI float bflo(unsigned u) { return __uint_as_float(u << 16); }
DI float bfhi(unsigned u) { return __uint_as_float(u & 0xffff0000u); }
DI float swap_add(float v) { auto r = __builtin_amdgcn_permlane32_swap(__float_as_uint(v), __float_as_uint(v), false, false); return __uint_as_float(r[0]) + __uint_as_float(r[1]); }
DI float swap_max(float v) { auto r = __builtin_amdgcn_permlane32_swap(__float_as_uint(v), __float_as_uint(v), false, false); return fmaxf(__uint_as_float(r[0]), __uint_as_float(r[1])); }
DI void glds16(const void* sbase, unsigned voff, unsigned lds_base) {
    asm volatile("s_mov_b32 m0, %2\n\ts_nop 0\n\tglobal_load_lds_dwordx4 %0, %1" :: "v"(voff), "s"(sbase), "s"(lds_base) : "memory");
}
DI void glds16c(const void* sbase, unsigned voff, unsigned lds_base) {
    asm volatile("s_mov_b32 m0, %2\n\ts_nop 0\n\tglobal_load_lds_dwordx4 %0, %1 sc1" :: "v"(voff), "s"(sbase), "s"(lds_base) : "memory");
}
DI u32x2 trd(LAS const char* p) { return __builtin_bit_cast(u32x2, __builtin_amdgcn_ds_read_tr16_b64_v4i16((LAS v4i16_t*)p)); }
DI u32x4 trfrag(LAS const char* p, int second) { u32x2 a = trd(p), b = trd(p + second); return u32x4{a.x, a.y, b.x, b.y}; }
DI u32x4 pack8(const f32x16& a, int s) {
    return u32x4{pk2(a[8 * s], a[8 * s + 1]), pk2(a[8 * s + 2], a[8 * s + 3]), pk2(a[8 * s + 4], a[8 * s + 5]), pk2(a[8 * s + 6], a[8 * s + 7])};
}
DI float max16(const f32x16& p) {
    float a = fmaxf(fmaxf(p[0], p[1]), p[2]), b = fmaxf(fmaxf(p[3], p[4]), p[5]);
    a = fmaxf(fmaxf(a, p[6]), p[7]); b = fmaxf(fmaxf(b, p[8]), p[9]);
    a = fmaxf(fmaxf(a, p[10]), p[11]); b = fmaxf(fmaxf(b, p[12]), p[13]);
    a = fmaxf(fmaxf(a, p[14]), p[15]);
    return fmaxf(a, b);
}
DI u32x2 split3(float m) {
    const float v = -m;
    const unsigned u1 = __float_as_uint(v) & 0xffff0000u; const float r1 = v - __uint_as_float(u1);
    const unsigned u2 = __float_as_uint(r1) & 0xffff0000u; const float r2 = r1 - __uint_as_float(u2);
    const unsigned u3 = __float_as_uint(r2) & 0xffff0000u;
    return u32x2{(u1 >> 16) | u2, u3 >> 16};
}
DI u32x4 corr_a(float cs, int hi) {
    const unsigned h = pk2(cs, cs); const float lo = cs - bflo(h); const unsigned l = pk2(lo, lo);
    return hi ? u32x4{0u, 0u, 0u, 0u} : u32x4{h, l, 0u, 0u};
}
DI u32x4 corr_b(float mu) {
    const unsigned h = pk2(-mu, 0.f); const unsigned d = pk2(-mu, -mu - bflo(h));
    return u32x4{d, d, 0u, 0u};
}
DI u32x4 ldf(__amdgpu_buffer_rsrc_t rs, int l16, int soff) { return __builtin_amdgcn_raw_buffer_load_b128(rs, l16, soff, SC1); }
#define LDK(rs_, base, k) ldf(rs_, l16 + ((k) & 3) * KB, (base) + ((k) >> 2) * 4 * KB)
#define SPIN(v) asm volatile("" : "+s"(v))
DI void stf(u32x4 v, __amdgpu_buffer_rsrc_t rs, int l16, int soff) {
    __builtin_amdgcn_raw_buffer_store_b128(v, rs, l16, soff, SC1);
    asm volatile("s_nop 1" :: "v"(v) : "memory");
}
DI void sth(u32x2 p, __amdgpu_buffer_rsrc_t rs, int lane, int w, int soff) {
    const auto a = __builtin_amdgcn_permlane32_swap(p.x, p.x, false, false), c = __builtin_amdgcn_permlane32_swap(p.y, p.y, false, false);
    if (lane < 32) stf(u32x4{p.x, p.y, a[1], c[1]}, rs, lane * 16 + (w & 1) * 512, soff);
}
DI void lds_wait(unsigned addr, unsigned n) {
    unsigned t;
    asm volatile("1:\n\tds_read_b32 %0, %1\n\ts_waitcnt lgkmcnt(0)\n\tv_cmp_gt_u32 vcc, %2, %0\n\ts_nop 4\n\ts_cbranch_vccnz 1b" : "=&v"(t) : "v"(addr), "s"(n) : "vcc", "memory");
}
DI unsigned flag_ld(char* ws, int word) { return __hip_atomic_load((GAS unsigned*)ws + word, RLX_AGENT); }
DI void flag_st(char* ws, int word, unsigned v) { __hip_atomic_store((GAS unsigned*)ws + word, v, RLX_AGENT); }
DI void poll_flags(char* ws, int word, unsigned tag, unsigned first) {
    unsigned v = first;
    for (unsigned spins = 0; !__all(v == tag) && spins < (1u << 17); ++spins) v = flag_ld(ws, word);
    __builtin_amdgcn_fence(__ATOMIC_ACQUIRE, "wavefront");
}

#define STAMP(kid, i)
#define STAMP_FLUSH()
constexpr int STAMP_BYTES = 0;

constexpr int A_KP = 2176, A_XW = 16 * 1040, A_SLOT = A_XW + 4 * A_KP, A_WQ = A_SLOT, A_PT = A_WQ + 28 * KB + 64, A_CS = 149 * KB;
static_assert(6 * A_SLOT <= A_CS && A_PT + 16 * KB <= 3 * A_SLOT, "phase 1 LDS");
constexpr int B_RED = 0;
constexpr int B_HS = 64 * KB;
constexpr int B_QS = 80 * KB;
constexpr int B_ST = 92 * KB;
constexpr int B_W1 = 96 * KB;
constexpr int B_FLAG = 152 * KB;
constexpr int STAMP_LDS = B_FLAG + 128;
constexpr int LDS_BYTES = STAMP_LDS + STAMP_BYTES;
static_assert(LDS_BYTES <= 160 * KB, "LDS");

DI void red_write(LAS char* lds, int w, int lane, const f32x16& a, const f32x16& c) {
    LAS f32x4* red = (LAS f32x4*)(lds + B_RED) + (w * 8) * 64 + lane;
#pragma unroll
    for (int g = 0; g < 4; ++g) {
        red[g * 64] = f32x4{a[4 * g], a[4 * g + 1], a[4 * g + 2], a[4 * g + 3]};
        red[(4 + g) * 64] = f32x4{c[4 * g], c[4 * g + 1], c[4 * g + 2], c[4 * g + 3]};
    }
}
DI f32x4 red_sum(LAS char* lds, int w, int lane) {
    LAS const f32x4* red = (LAS const f32x4*)(lds + B_RED) + ((w & 6) + (lane >> 5)) * 64 + 32 * (w & 1) + (lane & 31);
    f32x4 v[8];
#pragma unroll
    for (int i = 0; i < 8; ++i) v[i] = red[i * 8 * 64];
    SB();
    return ((v[0] + v[1]) + (v[2] + v[3])) + ((v[4] + v[5]) + (v[6] + v[7]));
}

__global__ __launch_bounds__(512, 2) void fused_block(const unsigned short* __restrict__ x, const unsigned short* __restrict__ wqkv, const unsigned short* __restrict__ wo,
                                                      const unsigned short* __restrict__ w1, const unsigned short* __restrict__ w2, char* __restrict__ ws, unsigned short* __restrict__ out) {
    extern __shared__ __attribute__((aligned(16))) char smem[];
    const int tid = threadIdx.x, lane = tid & 63, w = __builtin_amdgcn_readfirstlane(tid >> 6), r = lane & 31, hi = lane >> 5;
    const int b = blockIdx.x, xcd = b & 7, bi = b >> 3;
    unsigned sink;
    {
        unsigned long long pc; unsigned len;
        asm volatile(".Lfb_begin:\n\ts_getpc_b64 %0\n\ts_mov_b32 %1, .Lfb_end-.Lfb_begin" : "=s"(pc), "=s"(len));
        const unsigned off = (unsigned)(((lane & 1) * 8 + w) * 32 + bi) * 128u;
        asm volatile("global_load_dword %0, %1, %2" : "=v"(sink) : "v"(off < len && len < 65536u ? off : 0u), "s"(pc) : "memory");
    }
    asm volatile("" :: "s"(x), "s"(wqkv), "s"(ws));
    LAS char* lds = (LAS char*)smem;
    const unsigned lds0 = (unsigned)(uintptr_t)smem;
    const int h = (xcd & 3) * 4 + (bi & 3), rb = (xcd >> 2) * 8 + (bi >> 2);
    const int rot = (2 * (bi & 3) + (bi >> 2)) % 7;
    const int l16 = lane * 16;
    const int trq = (lane & 15) >> 2, trc = ((lane >> 4) & 1) * 32 + (lane & 3) * 8;
    const __amdgpu_buffer_rsrc_t rs = __builtin_amdgcn_make_buffer_rsrc((void*)ws, 0, WS_BYTES, 0x00020000);
    LAS float* csl = (LAS float*)(lds + A_CS);
    volatile LAS unsigned* lflag = (volatile LAS unsigned*)(lds + B_FLAG);
    STAMP(0, 0);
    unsigned tag;
    u32x4 q0 = {}, q1 = {};
    if (__builtin_expect(w >= 4, 1)) {
        const int L = w - 4;
        const char* xb = (const char*)x + (size_t)(rb * 128 + 64 * (L >> 1) + 4 * (L & 1)) * 896;
        const unsigned xo0 = (lane >> 3) * (8 * 896) + (lane & 7) * 16, xo1 = xo0 + 896, xo2 = xo0 + 2 * 896, xo3 = xo0 + 3 * 896;
        const char* wkv = (const char*)wqkv + (size_t)(16 * L) * 2688 + 896 + 112 * h;
        const unsigned woff = (lane >> 3) * (2 * 2688) + (lane & 7) * 16, woff2 = woff + 2688;
#pragma unroll 1
        for (int s = 0; s < 7; ++s) {
            int kb = s + rot; kb = kb >= 7 ? kb - 7 : kb;
            const unsigned d_ = lds0 + (s == 6 ? 0 : s) * A_SLOT;
            if (s == 6) { while (lflag[4] < 4u) __builtin_amdgcn_s_sleep(1); }
            glds16(xb + kb * 128, xo0, d_ + L * 4160); glds16(xb + kb * 128, xo1, d_ + L * 4160 + 1040);
            glds16(xb + kb * 128, xo2, d_ + L * 4160 + 2080); glds16(xb + kb * 128, xo3, d_ + L * 4160 + 3120);
            if ((lane & 7) != 7) { glds16(wkv + (size_t)kb * (64 * 2688), woff, d_ + A_XW + L * A_KP); glds16(wkv + (size_t)kb * (64 * 2688), woff2, d_ + A_XW + L * A_KP + 1088); }
            if (s == 0) BAR();
            else { VMWAIT(6); if (lane == 0) __hip_atomic_fetch_add((LAS unsigned*)&lflag[8 + s - 1], 1u, __ATOMIC_RELAXED, __HIP_MEMORY_SCOPE_WORKGROUP); if (s == 1) STAMP(0, 1); if (s == 4) STAMP(0, 2); }
        }
        VMWAIT(0);
        if (lane == 0) __hip_atomic_fetch_add((LAS unsigned*)&lflag[8 + 6], 1u, __ATOMIC_RELAXED, __HIP_MEMORY_SCOPE_WORKGROUP);
        STAMP(0, 3);
        PIN(sink);
        while (lflag[5] < 4u) __builtin_amdgcn_s_sleep(1);
        {
            const char* wq = (const char*)wqkv + (size_t)(16 * L) * 2688 + ((56 * h) & ~15);
            const unsigned qoff = (lane >> 2) * 2688 + (lane & 3) * 16;
#pragma unroll 1
            for (int i = 0; i < 7; ++i) { int kb = i + rot; kb = kb >= 7 ? kb - 7 : kb; glds16(wq + (size_t)kb * (64 * 2688), qoff, lds0 + A_WQ + (4 * i + L) * 1024); }
        }
        const char* psrc; int ppitch, pdst, pstep, k0;
        if (b < 112) { const int cp = b >> 2; k0 = (b & 3) * 7; psrc = (const char*)w1 + cp * 128; ppitch = FF * 2; pdst = W1_OFF + (cp * 2 * 28) * KB; pstep = 28 * KB; }
        else if (b < 224) { const int q = b - 112, cp = q >> 4; k0 = (q & 15) * 7; psrc = (const char*)w2 + cp * 128; ppitch = DM * 2; pdst = W2_OFF + (cp * 2 * 112) * KB; pstep = KB; }
        else { k0 = (b - 224) * 7; psrc = (const char*)wo; ppitch = DM * 2; pdst = WO_OFF; pstep = 4 * KB; }
        int pks[2], pd[2];
#pragma unroll
        for (int i = 0; i < 2; ++i) {
            const int kk = k0 + ((L + 4 * i < 7) ? L + 4 * i : L);
            const int cp = kk >> 5, ks = kk & 31;
            pks[i] = (b >= 224) ? (ks & 1) : 0; pd[i] = (b >= 224) ? pdst + (cp * 64 + (ks >> 2) * 8 + (ks & 3)) * KB : pdst + (b >= 112 ? 2 * kk : kk) * KB;
#pragma unroll
            for (int u = 0; u < 2; ++u) {
                const int row = 8 * u + (lane >> 3), c = 16 * (ks & 1) + row;
                const int srow = (b >= 224) ? (ks >> 1) * DH + (c < DH ? c : DH - 1) : 16 * kk + row;
                glds16(psrc, (unsigned)(srow * ppitch + ((b >= 224) ? cp * 128 : 0) + (lane & 7) * 16), lds0 + A_PT + ((L * 2 + i) * 2 + u) * 1024);
            }
        }
        VMWAIT(4);
        float cq = 0.f;
        LAS const char* tq = lds + A_WQ + L * 1024 + (8 * hi + trq) * 64 + trc + ((56 * h) & 15);
#pragma unroll
        for (int i = 0; i < 7; ++i) cq = sum8(trfrag(tq + i * 4096, 256), cq);
        csl[(2 * 32 + r) * 8 + 2 * L + hi] = cq;
        STAMP(0, 4);
        BAR();
        tag = __builtin_amdgcn_readfirstlane(lflag[2]);
        LAS const char* tp = lds + A_PT + L * 4096 + (4 * hi + trq) * 128 + trc;
        VMWAIT(0);
#pragma unroll
        for (int i = 0; i < 2; ++i) {
            if (L + 4 * i < 7) {
#pragma unroll
                for (int ct = 0; ct < 2; ++ct) {
                    u32x4 f = trfrag(tp + i * 2048 + ct * 64, 1024);
                    if (pks[i] && hi) { f.z = 0u; f.w = 0u; }
                    stf(f, rs, l16, pd[i] + ct * pstep);
                }
            }
        }
        STAMP(0, 5);
    } else {
        const int rt = rb * 4 + w;
        unsigned ftch = flag_ld(ws, ((b * 4 + w) & 255) * 32);
        const unsigned gen = flag_ld(ws, FW_GEN);
        if (tid < 32) lflag[tid] = 0u;
        if ((lane & 7) == 7) {
#pragma unroll
            for (int s = 0; s < 12; ++s) *(LAS u32x4*)(lds + (s >> 1) * A_SLOT + A_XW + w * A_KP + (s & 1) * 1088 + l16) = u32x4{ONES, ONES, ONES, ONES};
        }
        BAR();
        LAS const char* xr = lds + (8 * (w >> 1) + (r & 7)) * 1040 + (4 * (w & 1) + (r >> 3)) * 128 + hi * 16;
        LAS const char* tw = lds + A_XW + (4 * hi + (trq >> 1)) * 128 + (trq & 1) * 1088 + trc;
        u32x4 xf[28];
        f32x16 a0 = {}, a1 = {};
        float s2 = 0.f, c0 = 0.f, c1 = 0.f;
        u32x4 wa[2][4], wb[2][4];
#define A_WAIT(s) { while (lflag[8 + (s)] < 4u) {} asm volatile("" ::: "memory"); }
#define A_RDX(s, i) { xf[4 * (s) + i] = *(LAS const u32x4*)(xr + ((s) % 6) * A_SLOT + (i ^ w) * 32); wa[(s) & 1][i] = trfrag(tw + ((s) % 6) * A_SLOT + (i ^ w) * A_KP, 256); SB(); }
#define A_RDW(s, i) { wb[(s) & 1][i] = trfrag(tw + ((s) % 6) * A_SLOT + (i ^ w) * A_KP + 56, 256); SB(); }
#define A_STAGE(s, DONE) { if ((s) < 6) A_WAIT((s) + 1) \
        if ((DONE) && lane == 0) __hip_atomic_fetch_add((LAS unsigned*)&lflag[DONE], 1u, __ATOMIC_RELAXED, __HIP_MEMORY_SCOPE_WORKGROUP); \
        _Pragma("unroll") for (int i = 0; i < 4; ++i) { \
            a0 = MFMA(wa[(s) & 1][i], xf[4 * (s) + i], a0); PIN(a0); SB(); if ((s) < 6) A_RDX(((s) < 6 ? (s) + 1 : 6), i) \
            a1 = MFMA(wb[(s) & 1][i], xf[4 * (s) + i], a1); PIN(a1); SB(); if ((s) < 6) A_RDW(((s) < 6 ? (s) + 1 : 6), i) } \
        _Pragma("unroll") for (int i = 0; i < 4; ++i) s2 = sq8(xf[4 * (s) + i], s2); \
        c0 = sum8(wa[(s) & 1][0], c0); c1 = sum8(wb[(s) & 1][0], c1); PIN(s2); PIN(c0); PIN(c1); SB(); }
        A_WAIT(0)
#pragma unroll
        for (int i = 0; i < 4; ++i) { A_RDX(0, i) A_RDW(0, i) }
        A_STAGE(0, 4)
        STAMP(0, 1);
        A_STAGE(1, 0) A_STAGE(2, 5) A_STAGE(3, 0)
        STAMP(0, 2);
        A_STAGE(4, 0) A_STAGE(5, 0)
        csl[(0 * 32 + r) * 8 + 2 * w + hi] = c0; csl[(1 * 32 + r) * 8 + 2 * w + hi] = c1;
        asm volatile("s_waitcnt lgkmcnt(0)" ::: "memory");
        if (lane == 0) __hip_atomic_fetch_add((LAS unsigned*)&lflag[6], 1u, __ATOMIC_RELAXED, __HIP_MEMORY_SCOPE_WORKGROUP);
        c0 = 0.f; c1 = 0.f;
#pragma unroll
        for (int i = 0; i < 4; ++i) { s2 = sq8(xf[24 + i], s2); c0 = sum8(wa[0][i], c0); c1 = sum8(wb[0][i], c1); }
        PIN(s2); PIN(c0); PIN(c1); SB();
#pragma unroll
        for (int i = 0; i < 4; ++i) a1 = MFMA(wb[0][i], xf[24 + i], a1);
        PIN(a1); SB();
        while (lflag[6] < 4u) {}
        asm volatile("" ::: "memory");
#define A_CS(t) ({ const f32x4 p0 = *(LAS const f32x4*)(csl + ((t) * 32 + r) * 8), p1 = *(LAS const f32x4*)(csl + ((t) * 32 + r) * 8 + 4); ((p0.x + p0.y) + (p0.z + p0.w)) + ((p1.x + p1.y) + (p1.z + p1.w)); })
#pragma unroll
        for (int i = 0; i < 4; ++i) a0 = MFMA(wa[0][i], xf[24 + i], a0);
        STAMP(0, 3);
        tag = __builtin_amdgcn_readfirstlane(gen) + 1u;
        PIN(sink); PIN(ftch);
        if (tid == 0) lflag[2] = tag;
        s2 = swap_add(s2);
        const float mu = swap_add(hi ? a1[12] : 0.f) * (1.f / DM), var = fmaxf(s2 * (1.f / DM) - mu * mu, 0.f), rstd = __builtin_amdgcn_rsqf(var + EPS);
        const u32x4 cb = corr_b(mu);
        const int dst = KV_OFF + (((2 * h) & 15) * 64 + rt) * 4 * KB + (h < 8 ? 0 : 2 * KB);
        const float cs0 = A_CS(0) + swap_add(c0), cs1 = A_CS(1) + swap_add(c1);
        a1 = MFMA(corr_a(cs1, hi), cb, a1); a0 = MFMA(corr_a(cs0, hi), cb, a0);
        a0 *= rstd; a1 *= rstd;
        u32x4 f00 = pack8(a0, 0), f01 = pack8(a0, 1), f10 = pack8(a1, 0), f11 = pack8(a1, 1);
        const unsigned pz = h < 8 ? ONES : 0x00003f80u, pw = h < 8 ? 0x00003f80u : 0u;
        if (hi) { f01.z = pz; f01.w = pw; f11.z = pz; f11.w = pw; }
        if (h >= 8) {
            LAS char* tb = lds + 4 * A_SLOT + w * 4096;
            LAS char* wr = tb + r * 64 + hi * 8;
            *(LAS u32x2*)(wr) = u32x2{f00.x, f00.y}; *(LAS u32x2*)(wr + 16) = u32x2{f00.z, f00.w}; *(LAS u32x2*)(wr + 32) = u32x2{f01.x, f01.y}; *(LAS u32x2*)(wr + 48) = u32x2{f01.z, f01.w};
            *(LAS u32x2*)(wr + 2048) = u32x2{f10.x, f10.y}; *(LAS u32x2*)(wr + 2064) = u32x2{f10.z, f10.w}; *(LAS u32x2*)(wr + 2080) = u32x2{f11.x, f11.y}; *(LAS u32x2*)(wr + 2096) = u32x2{f11.z, f11.w};
            asm volatile("s_waitcnt lgkmcnt(0)" ::: "memory");
            LAS const char* rd = tb + (4 * hi + trq) * 64 + trc;
            f00 = trfrag(rd, 512); f01 = trfrag(rd + 1024, 512); f10 = trfrag(rd + 2048, 512); f11 = trfrag(rd + 3072, 512);
        }
        stf(f00, rs, l16, dst); stf(f01, rs, l16 + KB, dst); stf(f10, rs, l16, dst + 64 * 4 * KB); stf(f11, rs, l16 + KB, dst + 64 * 4 * KB);
        STAMP(0, 4);
        VMWAIT(0);
        if (lane == 0 && __hip_atomic_fetch_add((LAS unsigned*)&lflag[7], 1u, __ATOMIC_RELAXED, __HIP_MEMORY_SCOPE_WORKGROUP) == 3u) flag_st(ws, FW_KV + h * 32 + rb, tag);
        BAR();
        LAS const char* tq = lds + A_WQ + (8 * hi + trq) * 64 + trc + ((56 * h) & 15);
        f32x16 aq = {};
        u32x4 wq[8];
#pragma unroll
        for (int i = 0; i < 8; ++i) wq[i] = trfrag(tq + (i >> 2) * 4096 + ((i & 3) ^ w) * 1024, 256);
        SB();
#pragma unroll
        for (int i = 0; i < 28; ++i) {
            aq = MFMA(wq[i & 7], xf[i], aq); SB();
            if (i + 8 < 28) wq[i & 7] = trfrag(tq + ((i + 8) >> 2) * 4096 + ((i & 3) ^ w) * 1024, 256);
            SB();
        }
        aq = MFMA(corr_a(A_CS(2), hi), cb, aq);
        aq *= rstd * QSCALE;
        q0 = pack8(aq, 0); q1 = pack8(aq, 1);
        if (hi) { q1.z = 0u; q1.w = 0u; }
        STAMP(0, 5);
    }
    const bool post = bi < 28;
    const int gam = 4 * xcd + bi / 7, c7 = bi % 7;
    const int rtA = (gam >> 1) * 4 + (gam & 1);
    const __amdgpu_buffer_rsrc_t rw = __builtin_amdgcn_make_buffer_rsrc((void*)ws, 0, post ? WS_BYTES : 0, 0x00020000);
    u32x4 R[32];
    const int kvo = KV_OFF + (h * 64 + w * 8) * 4 * KB;
#define KVL(i) ldf(rs, l16 + ((i) & 3) * KB, kvo + (((((i) >> 2) + rb) & 7) << 12))
    {
        const int fw = FW_KV + ((h >> 1) + 8 * (lane & 1)) * 32 + 2 * w + ((lane >> 1) & 1);
        poll_flags(ws, fw, tag, tag + 1u);
        STAMP(0, 6);
#pragma unroll
        for (int i = 0; i < 8; ++i) { R[i] = KVL(i); SB(); }
        if (w < 4) {
            f32x16 sd = {};
            sd = MFMA(R[0], q0, sd); sd = MFMA(R[1], q1, sd);
            const float m = swap_max(max16(sd));
            if (hi) { const u32x2 p = split3(m); q1.z = p.x; q1.w = p.y; }
            *(LAS u32x4*)(lds + B_QS + w * 2048 + l16) = q0; *(LAS u32x4*)(lds + B_QS + w * 2048 + 1024 + l16) = q1;
        }
    }
    BAR();
    if (tid == 256) flag_st(ws, FW_W + b, tag);
    STAMP(0, 7);
    int woo = WO_OFF + (c7 * 8 + w) * 8 * KB, w1o = W1_OFF + (8 * c7 + w) * 28 * KB, w2o = W2_OFF + (c7 * 8 + w) * 28 * KB;
    int ofo = OF_OFF + (rtA * 32 + 4 * w) * KB, ufo = UF_OFF + (rtA * 112 + 14 * w) * KB;
    SPIN(woo); SPIN(w1o); SPIN(w2o); SPIN(ofo); SPIN(ufo);
#define EXP4(S, i) { S[i] = __builtin_amdgcn_exp2f(S[i]); S[i + 1] = __builtin_amdgcn_exp2f(S[i + 1]); S[i + 2] = __builtin_amdgcn_exp2f(S[i + 2]); S[i + 3] = __builtin_amdgcn_exp2f(S[i + 3]); }
#define HOOK(PASS, i) if ((i) >= 32) { glds16c(ws + w1o + (16 + (i) - 32) * KB, l16, lds0 + B_W1 + (w * 7 + (i) - 32) * KB); } \
        else if ((i) >= 0) R[(i) & 31] = (PASS) ? (((i) & 31) < 8 ? LDK(rw, woo, (i) & 31) : LDK(rw, w1o, ((i) & 31) - 8)) : KVL((i) & 31);
#define STEP(PASS, C, N, OC, OP, QN0, QN1, t, tn, tp, FIRST, LAST, L0, L1, L2, L3) { \
        if (!(LAST)) { N = MFMA(R[4 * (tn)], QN0, zero16); SB(); } \
        HOOK(PASS, L0) \
        EXP4(C, 0) p0.x = pk2(C[0], C[1]); p0.y = pk2(C[2], C[3]); PIN(p0); SB(); \
        if (!(FIRST)) { OP = MFMA(R[4 * (tp) + 3], p1, OP); SB(); } \
        HOOK(PASS, L1) \
        EXP4(C, 4) p0.z = pk2(C[4], C[5]); p0.w = pk2(C[6], C[7]); PIN(p0); SB(); \
        if (!(LAST)) { N = MFMA(R[4 * (tn) + 1], QN1, N); SB(); } \
        HOOK(PASS, L2) \
        EXP4(C, 8) p1.x = pk2(C[8], C[9]); p1.y = pk2(C[10], C[11]); PIN(p1); SB(); \
        OC = MFMA(R[4 * (t) + 2], p0, OC); SB(); \
        HOOK(PASS, L3) \
        EXP4(C, 12) p1.z = pk2(C[12], C[13]); p1.w = pk2(C[14], C[15]); PIN(p1); SB(); }
#define TILE(PASS, t, A0, A1, A2, A3, B0, B1, B2, B3) \
        STEP(PASS, sa, sb, oa, ob, qb0, qb1, t, t, (t) - 1 < 0 ? 0 : (t) - 1, (t) == 0, false, A0, A1, A2, A3) \
        STEP(PASS, sb, sa, ob, oa, qa0, qa1, t, (t) + 1 > 7 ? 7 : (t) + 1, t, false, (t) == 7, B0, B1, B2, B3)
#define TILE0(t) TILE(0, t, 4 * (t) + 8, -1, 4 * (t) + 9, -1, 4 * (t) + 10, -1, 4 * (t) + 11, -1)
#define COMBINE(PASS) { \
        LAS const float* lp = (LAS const float*)(lds + B_RED) + (((w >> 2) * 4 + 3) * 64 + 32 + r) * 4; \
        float lv[8]; _Pragma("unroll") for (int i = 0; i < 8; ++i) lv[i] = lp[i * 8 * 64 * 4]; \
        const f32x4 o = red_sum(lds, w, lane); \
        const float il = __builtin_amdgcn_rcpf(((lv[0] + lv[1]) + (lv[2] + lv[3])) + ((lv[4] + lv[5]) + (lv[6] + lv[7]))); \
        const int qt = rb * 4 + (PASS) * 2 + (w >> 2); \
        sth(u32x2{pk2(o[0] * il, o[1] * il), pk2(o[2] * il, o[3] * il)}, rs, lane, w, OF_OFF + ((qt * 16 + h) * 2 + ((w >> 1) & 1)) * KB); }
#define LADD(i) __hip_atomic_fetch_add((LAS unsigned*)&lflag[i], 1u, __ATOMIC_RELAXED, __HIP_MEMORY_SCOPE_WORKGROUP)
#define COMBINEL(u) { \
        LAS const float* lp = (LAS const float*)(lds + B_RED) + ((((u) >> 2) * 4 + 3) * 64 + 32 + r) * 4; \
        LAS const f32x4* cp = (LAS const f32x4*)(lds + B_RED) + (((u) & 6) + hi) * 64 + 32 * ((u) & 1) + r; \
        f32x4 o = (cp[0] + cp[512]) + (cp[1024] + cp[1536]); float l = (lp[0] + lp[2048]) + (lp[4096] + lp[6144]); PIN(o); PIN(l); SB(); \
        o += (cp[2048] + cp[2560]) + (cp[3072] + cp[3584]); l += (lp[8192] + lp[10240]) + (lp[12288] + lp[14336]); \
        const float il = __builtin_amdgcn_rcpf(l); \
        sth(u32x2{pk2(o[0] * il, o[1] * il), pk2(o[2] * il, o[3] * il)}, rs, lane, u, OF_OFF + (((rb * 4 + ((u) >> 2)) * 16 + h) * 2 + (((u) >> 1) & 1)) * KB); SB(); }
#define FLAG_O(PASS) { if (tid < 2) flag_st(ws, FW_RT + (0 * 64 + rb * 4 + (PASS) * 2 + tid) * 32 + h, tag); }
#define LOADQ(PASS) \
        qa0 = *(LAS const u32x4*)(lds + B_QS + (PASS) * 4096 + l16); qa1 = *(LAS const u32x4*)(lds + B_QS + (PASS) * 4096 + 1024 + l16); \
        qb0 = *(LAS const u32x4*)(lds + B_QS + (PASS) * 4096 + 2048 + l16); qb1 = *(LAS const u32x4*)(lds + B_QS + (PASS) * 4096 + 3072 + l16);
#define PASS_BEGIN(PASS) LOADQ(PASS) oa = zero16; ob = zero16; sa = MFMA(R[0], qa0, zero16); sa = MFMA(R[1], qa1, sa); SB();
#define PASS_END(PASS) \
        ob = MFMA(R[31], p1, ob); \
        { const bool bad = hi && !(oa[12] < 1.0e18f && ob[12] < 1.0e18f); \
          if (__any(bad) && lane == 0) lflag[PASS] = 1u; }
    u32x2 xres0, xres1;
    unsigned pfo;
    {
        const f32x16 zero16 = {};
        u32x4 qa0, qa1, qb0, qb1, p0, p1; f32x16 oa, ob, sa, sb;
        __builtin_amdgcn_s_setprio(3);
        PASS_BEGIN(0)
        TILE0(0) TILE0(1) TILE0(2) TILE0(3)
        __builtin_amdgcn_s_setprio(2);
        TILE0(4) TILE0(5)
        TILE(0, 6, -1, -1, -1, -1, -1, -1, -1, -1)
        const u32x4 wfl = __builtin_amdgcn_raw_buffer_load_b128(rs, l16, FW_W * 4, SC1);
        TILE(0, 7, -1, -1, -1, -1, -1, -1, -1, -1)
        STAMP(1, 0);
        __builtin_amdgcn_s_setprio(3);
        PASS_END(0)
        red_write(lds, w, lane, oa, ob);
        asm volatile("s_waitcnt lgkmcnt(0)" ::: "memory");
        if (lane == 0) LADD(16);
        {
            u32x4 v = wfl;
            for (unsigned spins = 0; !__all(v.x == tag && v.y == tag && v.z == tag && v.w == tag) && spins < (1u << 17); ++spins) {
                asm volatile("" ::: "memory");
                v = __builtin_amdgcn_raw_buffer_load_b128(rs, l16, FW_W * 4, SC1);
            }
            __builtin_amdgcn_fence(__ATOMIC_ACQUIRE, "wavefront");
        }
        PASS_BEGIN(1)
        __builtin_amdgcn_s_setprio(1);
        TILE(1, 0, 0, 32, 1, 33, 34, 35, 36, 2)
        TILE(1, 1, 4, 3, 5, 37, 38, -1, -1, 6)
        TILE(1, 2, 8, 7, 9, -1, -1, -1, -1, -1)
        TILE(1, 3, -1, -1, -1, -1, -1, -1, -1, -1)
        STAMP(1, 1);
        __builtin_amdgcn_s_setprio(0);
        if (w < 4) {
            lds_wait(lds0 + B_FLAG + 64, 8u);
            __builtin_amdgcn_s_setprio(3);
            const bool okA = lflag[0] == 0u;
            if (okA) { COMBINEL(w) COMBINEL(w + 4) }
            asm volatile("s_waitcnt lgkmcnt(0)" ::: "memory");
            if (lane == 0) LADD(17);
            VMWAIT(0);
            if (lane == 0 && LADD(18) == 3u && okA) { flag_st(ws, FW_RT + (rb * 4) * 32 + h, tag); flag_st(ws, FW_RT + (rb * 4 + 1) * 32 + h, tag); }
            __builtin_amdgcn_s_setprio(0);
        }
        STAMP(1, 2);
        TILE(1, 4, 10, 11, 12, 13, 14, 15, 16, 17)
        TILE(1, 5, 18, -1, 19, -1, 20, 21, -1, 22)
        {
            const unsigned short* xp = x + (size_t)((post ? rtA : 0) * 32 + r) * DM + c7 * 64 + (w >> 1) * 16 + 8 * hi + 4 * (w & 1);
            xres0 = *(const u32x2*)xp; xres1 = *(const u32x2*)(xp + 64 * DM);
        }
        TILE(1, 6, -1, 23, -1, -1, -1, -1, -1, -1)
        pfo = flag_ld(ws, FW_RT + (0 * 64 + (post ? rtA : 0)) * 32 + 2 * w + (lane & 1));
        TILE(1, 7, -1, -1, -1, -1, -1, -1, -1, -1)
        STAMP(1, 3);
        PASS_END(1)
        lds_wait(lds0 + B_FLAG + 68, 4u);
        red_write(lds, w, lane, oa, ob);
        BAR();
        const bool fb = lflag[0] != 0u || lflag[1] != 0u;
        if (__builtin_expect(fb, 0)) {
            if (lflag[1] == 0u) COMBINE(1)
#pragma unroll 1
            for (int p = 0; p < 2; ++p) {
                if (lflag[p] == 0u) continue;
                LOADQ(p)
                float ma = -INFINITY, mb = -INFINITY;
#pragma unroll 1
                for (int t = 0; t < 8; ++t) {
                    const u32x4 k0 = ldf(rs, l16, kvo + t * 4 * KB), k1 = ldf(rs, l16, kvo + t * 4 * KB + KB);
                    sa = MFMA(k0, qa0, zero16); sa = MFMA(k1, qa1, sa); sb = MFMA(k0, qb0, zero16); sb = MFMA(k1, qb1, sb);
                    ma = fmaxf(ma, max16(sa)); mb = fmaxf(mb, max16(sb));
                }
                ma = swap_max(ma); mb = swap_max(mb);
                LAS float* mx = (LAS float*)(lds + B_ST);
                mx[w * 64 + lane] = hi ? mb : ma;
                BAR();
#pragma unroll
                for (int i = 0; i < 8; ++i) { ma = fmaxf(ma, mx[i * 64 + r]); mb = fmaxf(mb, mx[i * 64 + 32 + r]); }
                if (hi) {
                    const u32x2 pa = split3(ma - (bflo(qa1.z) + bfhi(qa1.z) + bflo(qa1.w))), pb = split3(mb - (bflo(qb1.z) + bfhi(qb1.z) + bflo(qb1.w)));
                    qa1.z = pa.x; qa1.w = pa.y; qb1.z = pb.x; qb1.w = pb.y;
                }
                oa = zero16; ob = zero16;
#pragma unroll 1
                for (int t = 0; t < 8; ++t) {
                    const u32x4 k0 = ldf(rs, l16, kvo + t * 4 * KB), k1 = ldf(rs, l16, kvo + t * 4 * KB + KB), v0 = ldf(rs, l16, kvo + t * 4 * KB + 2 * KB), v1 = ldf(rs, l16, kvo + t * 4 * KB + 3 * KB);
                    sa = MFMA(k0, qa0, zero16); sa = MFMA(k1, qa1, sa); sb = MFMA(k0, qb0, zero16); sb = MFMA(k1, qb1, sb);
                    EXP4(sa, 0) EXP4(sa, 4) EXP4(sa, 8) EXP4(sa, 12) EXP4(sb, 0) EXP4(sb, 4) EXP4(sb, 8) EXP4(sb, 12)
                    oa = MFMA(v0, pack8(sa, 0), oa); oa = MFMA(v1, pack8(sa, 1), oa); ob = MFMA(v0, pack8(sb, 0), ob); ob = MFMA(v1, pack8(sb, 1), ob);
                }
                red_write(lds, w, lane, oa, ob);
                BAR();
                COMBINE(p)
                VMBAR(0);
                if (p == 0) FLAG_O(0)
            }
        }
        if (post) {
            poll_flags(ws, FW_RT + (0 * 64 + rtA) * 32 + 2 * w + (lane & 1), tag, pfo);
#pragma unroll
            for (int i = 0; i < 4; ++i) R[24 + i] = LDK(rs, ofo, i);
        }
        if (!fb) COMBINE(1)
    }
    if (!post) { VMBAR(0); FLAG_O(1) STAMP_FLUSH(); return; }
    const int rtB = rtA + 2;
    u32x4 E[5], of[4];
    f32x16 a0, a1;
#define WO_LOAD(rt) { _Pragma("unroll") for (int i = 0; i < 4; ++i) of[i] = LDK(rs, ofo + 64 * KB, i); SB(); }
#define WO_MFMA() { a0 = f32x16{}; a1 = f32x16{}; _Pragma("unroll") for (int i = 0; i < 4; ++i) { a0 = MFMA(R[i], of[i], a0); a1 = MFMA(R[4 + i], of[i], a1); } }
#define WO_FIN(rt, xr, hr) { const f32x4 v = red_sum(lds, w, lane); \
        hr = u32x2{pk2(v[0] + bflo(xr.x), v[1] + bfhi(xr.x)), pk2(v[2] + bflo(xr.y), v[3] + bfhi(xr.y))}; \
        sth(hr, rs, lane, w, HF_OFF + ((rt) * 28 + c7 * 4 + (w >> 1)) * KB); }
#define W2R(i) ((i) < 8 ? 24 + (i) : (i) - 8)
#define W2_LOAD(lo, hi_) { _Pragma("unroll") for (int i = (lo); i < (hi_); ++i) R[W2R(i)] = LDK(rs, w2o, i); SB(); }
    u32x2 hres0, hres1;
#pragma unroll
    for (int i = 0; i < 4; ++i) of[i] = R[24 + i];
    WO_MFMA()
    VMBAR(0);
    FLAG_O(1)
    STAMP(1, 4);
#pragma unroll
    for (int i = 0; i < 5; ++i) E[i] = LDK(rs, w1o, 23 + i);
    W2_LOAD(0, 4)
    red_write(lds, w, lane, a0, a1);
    BAR();
    WO_FIN(rtA, xres0, hres0)
    STAMP(1, 5);
    float cs1 = 0.f;
    LAS const char* w1l = lds + B_W1 + w * 7 * KB + l16;
#pragma unroll
    for (int i = 0; i < 16; ++i) cs1 = sum8(R[8 + i], cs1);
#pragma unroll
    for (int i = 0; i < 7; ++i) cs1 = sum8(*(LAS const u32x4*)(w1l + i * KB), cs1);
    PIN(cs1); SB();
    poll_flags(ws, FW_RT + (0 * 64 + rtB) * 32 + 2 * w + (lane & 1), tag, tag + 1u);
    WO_LOAD(rtB)
    BAR();
    if (tid == 0) flag_st(ws, FW_RT + (1 * 64 + rtA) * 32 + c7, tag);
    STAMP(1, 6);
    W2_LOAD(4, 8)
    WO_MFMA()
    red_write(lds, w, lane, a0, a1);
    BAR();
    WO_FIN(rtB, xres1, hres1)
    STAMP(1, 7);
#pragma unroll
    for (int i = 0; i < 5; ++i) cs1 = sum8(E[i], cs1);
    const u32x4 ca = corr_a(swap_add(cs1), hi);
    const int wp = w < 7 ? w : 6;
#define H_LOAD(rt) { _Pragma("unroll") for (int i = 0; i < 4; ++i) of[i] = LDK(rs, HF_OFF + ((rt) * 28 + 4 * wp) * KB, i); SB(); }
#define H_STAGE(hs, ab) { float s1 = 0.f, s2 = 0.f; \
        _Pragma("unroll") for (int i = 0; i < 4; ++i) { *(LAS u32x4*)(lds + (hs) + l16 + (4 * wp + i) * KB) = of[i]; s1 = sum8(of[i], s1); s2 = sq8(of[i], s2); } \
        s1 = swap_add(s1); s2 = swap_add(s2); ((LAS f32x2*)(lds + B_ST))[(ab) * 224 + wp * 32 + r] = f32x2{s1, s2}; }
    float rstd2;
#define F1_MFMA(hs, ab, HOOK) { u32x4 hb[8], wl[4], cb2; f32x2 sv[7]; a0 = f32x16{}; \
        _Pragma("unroll") for (int i = 0; i < 8; ++i) hb[i] = *(LAS const u32x4*)(lds + (hs) + l16 + i * KB); \
        SB(); \
        _Pragma("unroll") for (int i = 0; i < 28; ++i) { \
            a0 = MFMA(i < 16 ? R[8 + (i < 16 ? i : 0)] : i < 23 ? wl[i & 3] : E[i < 23 ? 0 : i - 23], hb[i & 7], a0); SB(); \
            if (i + 8 < 28) hb[i & 7] = *(LAS const u32x4*)(lds + (hs) + l16 + (i + 8) * KB); \
            if (i + 4 >= 16 && i + 4 < 23) wl[i & 3] = *(LAS const u32x4*)(w1l + (i + 4 - 16) * KB); \
            if (i == 20) { _Pragma("unroll") for (int k = 0; k < 7; ++k) sv[k] = ((LAS const f32x2*)(lds + B_ST))[(ab) * 224 + k * 32 + r]; } \
            if (i == 24) { const f32x2 t = ((sv[0] + sv[1]) + (sv[2] + sv[3])) + ((sv[4] + sv[5]) + sv[6]); const float mu = t.x * (1.f / DM); \
                rstd2 = __builtin_amdgcn_rsqf(fmaxf(t.y * (1.f / DM) - mu * mu, 0.f) + EPS); cb2 = corr_b(mu); PIN(cb2); PIN(rstd2); } \
            HOOK; SB(); } \
        a0 = MFMA(ca, cb2, a0); SB(); }
#define F1_FIN(rt) { const float nr = -LOG2E * rstd2; const int ud = UF_OFF + ((rt) * 112 + (8 * c7 + w) * 2) * KB; \
        _Pragma("unroll") for (int i = 0; i < 8; ++i) a0[i] = a0[i] * rstd2 * __builtin_amdgcn_rcpf(1.f + __builtin_amdgcn_exp2f(a0[i] * nr)); \
        stf(pack8(a0, 0), rs, l16, ud); \
        _Pragma("unroll") for (int i = 8; i < 16; ++i) a0[i] = a0[i] * rstd2 * __builtin_amdgcn_rcpf(1.f + __builtin_amdgcn_exp2f(a0[i] * nr)); \
        stf(pack8(a0, 1), rs, l16 + KB, ud); }
    poll_flags(ws, FW_RT + (1 * 64 + rtA) * 32 + wp, tag, tag + 1u);
    H_LOAD(rtA)
    H_STAGE(B_HS, 0)
    BAR();
    if (tid == 0) flag_st(ws, FW_RT + (1 * 64 + rtB) * 32 + c7, tag);
    STAMP(2, 0);
    unsigned pf;
    F1_MFMA(B_HS, 0, if (i < 8) R[i] = LDK(rs, w2o, i + 8); if (i == 20) pf = flag_ld(ws, FW_RT + (1 * 64 + rtB) * 32 + wp))
    STAMP(2, 1);
    poll_flags(ws, FW_RT + (1 * 64 + rtB) * 32 + wp, tag, pf);
    H_LOAD(rtB)
    STAMP(2, 2);
    F1_FIN(rtA)
    H_STAGE(B_RED, 1)
    STAMP(2, 3);
    VMBAR(0);
    if (tid == 0) flag_st(ws, FW_RT + (2 * 64 + rtA) * 32 + c7, tag);
    STAMP(2, 4);
    F1_MFMA(B_RED, 1, if (i < 12) R[8 + (i < 12 ? i : 0)] = LDK(rs, w2o, i + 16))
    STAMP(2, 5);
    const int fu = (7 * w + 6 * (lane & 1)) >> 3;
    pf = flag_ld(ws, FW_RT + (2 * 64 + rtA) * 32 + fu);
    F1_FIN(rtB)
    STAMP(2, 6);
    VMBAR(0);
    if (tid == 0) flag_st(ws, FW_RT + (2 * 64 + rtB) * 32 + c7, tag);
    STAMP(2, 7);
    u32x4 uf[8];
    f32x16 b0, b1;
#define U_LD(j) uf[(j) & 7] = LDK(rs, ufo + ((j) / 14) * 224 * KB, (j) % 14)
#define F2_FIN(red_, hr, o_) { const f32x4 v = red_sum(lds + (red_), w, lane); \
        o_ = u32x2{pk2(v[0] + bflo(hr.x), v[1] + bfhi(hr.x)), pk2(v[2] + bflo(hr.y), v[3] + bfhi(hr.y))}; }
    poll_flags(ws, FW_RT + (2 * 64 + rtA) * 32 + fu, tag, pf);
#pragma unroll
    for (int j = 0; j < 8; ++j) U_LD(j);
    SB();
    a0 = f32x16{}; a1 = f32x16{};
#pragma unroll
    for (int j = 0; j < 6; ++j) {
        a0 = MFMA(R[W2R(2 * j)], uf[j & 7], a0); a1 = MFMA(R[W2R(2 * j + 1)], uf[j & 7], a1); SB(); U_LD(j + 8);
        if (j == 3) pf = flag_ld(ws, FW_RT + (2 * 64 + rtB) * 32 + fu);
        SB();
    }
    STAMP(3, 0);
    poll_flags(ws, FW_RT + (2 * 64 + rtB) * 32 + fu, tag, pf);
    STAMP(3, 1);
#pragma unroll
    for (int j = 6; j < 14; ++j) { a0 = MFMA(R[W2R(2 * j)], uf[j & 7], a0); a1 = MFMA(R[W2R(2 * j + 1)], uf[j & 7], a1); SB(); U_LD(j + 8); SB(); }
    red_write(lds, w, lane, a0, a1);
    SB();
    b0 = f32x16{}; b1 = f32x16{};
#pragma unroll
    for (int j = 14; j < 28; ++j) { b0 = MFMA(R[W2R(2 * (j - 14))], uf[j & 7], b0); b1 = MFMA(R[W2R(2 * (j - 14) + 1)], uf[j & 7], b1); SB(); if (j + 8 < 28) U_LD(j + 8 < 28 ? j + 8 : 27); SB(); }
    STAMP(3, 2);
    red_write(lds + 64 * KB, w, lane, b0, b1);
    BAR();
    u32x2 oA, oB;
    F2_FIN(0, hres0, oA)
    F2_FIN(64 * KB, hres1, oB)
    unsigned short* op = out + (size_t)(rtA * 32 + r) * DM + c7 * 64 + (w >> 1) * 16 + 8 * hi + 4 * (w & 1);
    *(u32x2*)op = oA; *(u32x2*)(op + 64 * DM) = oB;
    STAMP(3, 3);
    if (b == 0 && tid == 0) flag_st(ws, FW_GEN, tag);
    STAMP_FLUSH();
    asm volatile(".Lfb_end:" ::: "memory");
}

extern "C" void kernel_launch(void* const* d_in, const int* in_sizes, int n_in, void* d_out, int out_size, void* d_ws, size_t ws_size, hipStream_t stream) {
    (void)hipFuncSetAttribute((const void*)fused_block, hipFuncAttributeMaxDynamicSharedMemorySize, LDS_BYTES);
    fused_block<<<256, 512, LDS_BYTES, stream>>>((const unsigned short*)d_in[0], (const unsigned short*)d_in[1], (const unsigned short*)d_in[2], (const unsigned short*)d_in[4],
                                                 (const unsigned short*)d_in[5], (char*)d_ws, (unsigned short*)d_out);
}
```

```cpp
#include <hip/hip_runtime.h>
#include <hip/hip_bf16.h>
#include <stdint.h>

typedef short bf16x8 __attribute__((ext_vector_type(8)));
typedef float f32x16 __attribute__((ext_vector_type(16)));
typedef float f32x4 __attribute__((ext_vector_type(4)));
typedef float f32x2 __attribute__((ext_vector_type(2)));
typedef unsigned u32x4 __attribute__((ext_vector_type(4)));
typedef unsigned u32x2 __attribute__((ext_vector_type(2)));
typedef __bf16 bf16x2_t __attribute__((ext_vector_type(2)));
typedef short v4i16_t __attribute__((ext_vector_type(4)));
#define LAS __attribute__((address_space(3)))
#define GAS __attribute__((address_space(1)))
#define DI __device__ __forceinline__
#define MFMA(a, b, c) __builtin_amdgcn_mfma_f32_32x32x16_bf16(__builtin_bit_cast(bf16x8, (a)), __builtin_bit_cast(bf16x8, (b)), (c), 0, 0, 0)
#define BAR() asm volatile("s_waitcnt lgkmcnt(0)\n\ts_barrier" ::: "memory")
#define VMBAR(N) asm volatile("s_waitcnt vmcnt(" #N ") lgkmcnt(0)\n\ts_barrier" ::: "memory")
#define VMWAIT(N) asm volatile("s_waitcnt vmcnt(" #N ")" ::: "memory")
#define SB() __builtin_amdgcn_sched_barrier(0)
#define PIN(v) asm volatile("" : "+v"(v))
#define RLX_AGENT __ATOMIC_RELAXED, __HIP_MEMORY_SCOPE_AGENT

constexpr int SEQ = 2048, DM = 448, NH = 16, DH = 28, FF = 1792;
constexpr float EPS = 1e-5f;
constexpr float LOG2E = 1.4426950408889634f;
constexpr float QSCALE = 0.18898223650461362f * LOG2E;
constexpr unsigned ONES = 0x3f803f80u;
constexpr int KB = 1024;
constexpr int FW_GEN = 0, FW_KV = 64, FW_W = FW_KV + 16 * 32, FW_RT = FW_W + 256;
constexpr int KV_OFF = 32 * KB;
constexpr int OF_OFF = KV_OFF + 16 * 64 * 4 * KB;
constexpr int WO_OFF = OF_OFF + 64 * 32 * KB;
constexpr int HF_OFF = WO_OFF + 14 * 32 * KB;
constexpr int W1_OFF = HF_OFF + 64 * 28 * KB;
constexpr int UF_OFF = W1_OFF + 56 * 28 * KB;
constexpr int W2_OFF = UF_OFF + 64 * 112 * KB;
constexpr int WS_BYTES = W2_OFF + 14 * 112 * KB;
constexpr int SC1 = 16;
static_assert((FW_RT + 3 * 64 * 32) * 4 <= KV_OFF, "flag block");

DI unsigned pk2(float lo, float hi) { f32x2 v = {lo, hi}; return __builtin_bit_cast(unsigned, __builtin_convertvector(v, bf16x2_t)); }
DI float dot2(unsigned a, unsigned b, float c) { return __builtin_amdgcn_fdot2_f32_bf16(__builtin_bit_cast(bf16x2_t, a), __builtin_bit_cast(bf16x2_t, b), c, false); }
DI float sum8(const u32x4 f, float c) { return dot2(f.w, ONES, dot2(f.z, ONES, dot2(f.y, ONES, dot2(f.x, ONES, c)))); }
DI float sq8(const u32x4 f, float c) { return dot2(f.w, f.w, dot2(f.z, f.z, dot2(f.y, f.y, dot2(f.x, f.x, c)))); }
DI float bflo(unsigned u) { return __uint_as_float(u << 16); }
DI float bfhi(unsigned u) { return __uint_as_float(u & 0xffff0000u); }
DI float swap_add(float v) { auto r = __builtin_amdgcn_permlane32_swap(__float_as_uint(v), __float_as_uint(v), false, false); return __uint_as_float(r[0]) + __uint_as_float(r[1]); }
DI float swap_max(float v) { auto r = __builtin_amdgcn_permlane32_swap(__float_as_uint(v), __float_as_uint(v), false, false); return fmaxf(__uint_as_float(r[0]), __uint_as_float(r[1])); }
DI void glds16(const void* sbase, unsigned voff, unsigned lds_base) {
    asm volatile("s_mov_b32 m0, %2\n\ts_nop 0\n\tglobal_load_lds_dwordx4 %0, %1" :: "v"(voff), "s"(sbase), "s"(lds_base) : "memory");
}
DI void glds16c(const void* sbase, unsigned voff, unsigned lds_base) {
    asm volatile("s_mov_b32 m0, %2\n\ts_nop 0\n\tglobal_load_lds_dwordx4 %0, %1 sc1" :: "v"(voff), "s"(sbase), "s"(lds_base) : "memory");
}
DI u32x2 trd(LAS const char* p) { return __builtin_bit_cast(u32x2, __builtin_amdgcn_ds_read_tr16_b64_v4i16((LAS v4i16_t*)p)); }
DI u32x4 trfrag(LAS const char* p, int second) { u32x2 a = trd(p), b = trd(p + second); return u32x4{a.x, a.y, b.x, b.y}; }
DI u32x4 pack8(const f32x16& a, int s) {
    return u32x4{pk2(a[8 * s], a[8 * s + 1]), pk2(a[8 * s + 2], a[8 * s + 3]), pk2(a[8 * s + 4], a[8 * s + 5]), pk2(a[8 * s + 6], a[8 * s + 7])};
}
DI float max16(const f32x16& p) {
    float a = fmaxf(fmaxf(p[0], p[1]), p[2]), b = fmaxf(fmaxf(p[3], p[4]), p[5]);
    a = fmaxf(fmaxf(a, p[6]), p[7]); b = fmaxf(fmaxf(b, p[8]), p[9]);
    a = fmaxf(fmaxf(a, p[10]), p[11]); b = fmaxf(fmaxf(b, p[12]), p[13]);
    a = fmaxf(fmaxf(a, p[14]), p[15]);
    return fmaxf(a, b);
}
DI u32x2 split3(float m) {
    const float v = -m;
    const unsigned u1 = __float_as_uint(v) & 0xffff0000u; const float r1 = v - __uint_as_float(u1);
    const unsigned u2 = __float_as_uint(r1) & 0xffff0000u; const float r2 = r1 - __uint_as_float(u2);
    const unsigned u3 = __float_as_uint(r2) & 0xffff0000u;
    return u32x2{(u1 >> 16) | u2, u3 >> 16};
}
DI u32x4 corr_a(float cs, int hi) {
    const unsigned h = pk2(cs, cs); const float lo = cs - bflo(h); const unsigned l = pk2(lo, lo);
    return hi ? u32x4{0u, 0u, 0u, 0u} : u32x4{h, l, 0u, 0u};
}
DI u32x4 corr_b(float mu) {
    const unsigned h = pk2(-mu, 0.f); const unsigned d = pk2(-mu, -mu - bflo(h));
    return u32x4{d, d, 0u, 0u};
}
DI u32x4 ldf(__amdgpu_buffer_rsrc_t rs, int l16, int soff) { return __builtin_amdgcn_raw_buffer_load_b128(rs, l16, soff, SC1); }
#define LDK(rs_, base, k) ldf(rs_, l16 + ((k) & 3) * KB, (base) + ((k) >> 2) * 4 * KB)
#define SPIN(v) asm volatile("" : "+s"(v))
DI void stf(u32x4 v, __amdgpu_buffer_rsrc_t rs, int l16, int soff) {
    __builtin_amdgcn_raw_buffer_store_b128(v, rs, l16, soff, SC1);
    asm volatile("s_nop 1" :: "v"(v) : "memory");
}
DI void sth(u32x2 p, __amdgpu_buffer_rsrc_t rs, int lane, int w, int soff) {
    const auto a = __builtin_amdgcn_permlane32_swap(p.x, p.x, false, false), c = __builtin_amdgcn_permlane32_swap(p.y, p.y, false, false);
    if (lane < 32) stf(u32x4{p.x, p.y, a[1], c[1]}, rs, lane * 16 + (w & 1) * 512, soff);
}
DI void lds_wait(unsigned addr, unsigned n) {
    unsigned t;
    asm volatile("1:\n\tds_read_b32 %0, %1\n\ts_waitcnt lgkmcnt(0)\n\tv_cmp_gt_u32 vcc, %2, %0\n\ts_nop 4\n\ts_cbranch_vccnz 1b" : "=&v"(t) : "v"(addr), "s"(n) : "vcc", "memory");
}
DI unsigned flag_ld(char* ws, int word) { return __hip_atomic_load((GAS unsigned*)ws + word, RLX_AGENT); }
DI void flag_st(char* ws, int word, unsigned v) { __hip_atomic_store((GAS unsigned*)ws + word, v, RLX_AGENT); }
DI void poll_flags(char* ws, int word, unsigned tag, unsigned first) {
    unsigned v = first;
    for (unsigned spins = 0; !__all(v == tag) && spins < (1u << 17); ++spins) v = flag_ld(ws, word);
    __builtin_amdgcn_fence(__ATOMIC_ACQUIRE, "wavefront");
}


constexpr int A_KP = 2176, A_XW = 16 * 1040, A_SLOT = A_XW + 4 * A_KP, A_WQ = A_SLOT, A_PT = A_WQ + 28 * KB + 64, A_CS = 149 * KB;
static_assert(6 * A_SLOT <= A_CS && A_PT + 16 * KB <= 3 * A_SLOT, "phase 1 LDS");
constexpr int B_RED = 0;
constexpr int B_HS = 64 * KB;
constexpr int B_QS = 80 * KB;
constexpr int B_ST = 92 * KB;
constexpr int B_W1 = 96 * KB;
constexpr int B_FLAG = 152 * KB;
constexpr int LDS_BYTES = B_FLAG + 128;
static_assert(LDS_BYTES <= 160 * KB, "LDS");

DI void red_write(LAS char* lds, int w, int lane, const f32x16& a, const f32x16& c) {
    LAS f32x4* red = (LAS f32x4*)(lds + B_RED) + (w * 8) * 64 + lane;
#pragma unroll
    for (int g = 0; g < 4; ++g) {
        red[g * 64] = f32x4{a[4 * g], a[4 * g + 1], a[4 * g + 2], a[4 * g + 3]};
        red[(4 + g) * 64] = f32x4{c[4 * g], c[4 * g + 1], c[4 * g + 2], c[4 * g + 3]};
    }
}
DI f32x4 red_sum(LAS char* lds, int w, int lane) {
    LAS const f32x4* red = (LAS const f32x4*)(lds + B_RED) + ((w & 6) + (lane >> 5)) * 64 + 32 * (w & 1) + (lane & 31);
    f32x4 v[8];
#pragma unroll
    for (int i = 0; i < 8; ++i) v[i] = red[i * 8 * 64];
    SB();
    return ((v[0] + v[1]) + (v[2] + v[3])) + ((v[4] + v[5]) + (v[6] + v[7]));
}

__global__ __launch_bounds__(512, 2) void fused_block(const unsigned short* __restrict__ x, const unsigned short* __restrict__ wqkv, const unsigned short* __restrict__ wo,
                                                      const unsigned short* __restrict__ w1, const unsigned short* __restrict__ w2, char* __restrict__ ws, unsigned short* __restrict__ out) {
    extern __shared__ __attribute__((aligned(16))) char smem[];
    const int tid = threadIdx.x, lane = tid & 63, w = __builtin_amdgcn_readfirstlane(tid >> 6), r = lane & 31, hi = lane >> 5;
    const int b = blockIdx.x, xcd = b & 7, bi = b >> 3;
    asm volatile("" :: "s"(x), "s"(wqkv), "s"(ws));
    LAS char* lds = (LAS char*)smem;
    const unsigned lds0 = (unsigned)(uintptr_t)smem;
    const int h = (xcd & 3) * 4 + (bi & 3), rb = (xcd >> 2) * 8 + (bi >> 2);
    const int rot = (2 * (bi & 3) + (bi >> 2)) % 7;
    const int l16 = lane * 16;
    const int trq = (lane & 15) >> 2, trc = ((lane >> 4) & 1) * 32 + (lane & 3) * 8;
    const __amdgpu_buffer_rsrc_t rs = __builtin_amdgcn_make_buffer_rsrc((void*)ws, 0, WS_BYTES, 0x00020000);
    LAS float* csl = (LAS float*)(lds + A_CS);
    volatile LAS unsigned* lflag = (volatile LAS unsigned*)(lds + B_FLAG);
    unsigned tag;
    u32x4 q0 = {}, q1 = {};
    if (__builtin_expect(w >= 4, 1)) {
        const int L = w - 4;
        const char* xb = (const char*)x + (size_t)(rb * 128 + 64 * (L >> 1) + 4 * (L & 1)) * 896;
        const unsigned xo0 = (lane >> 3) * (8 * 896) + (lane & 7) * 16, xo1 = xo0 + 896, xo2 = xo0 + 2 * 896, xo3 = xo0 + 3 * 896;
        const char* wkv = (const char*)wqkv + (size_t)(16 * L) * 2688 + 896 + 112 * h;
        const unsigned woff = (lane >> 3) * (2 * 2688) + (lane & 7) * 16, woff2 = woff + 2688;
#pragma unroll 1
        for (int s = 0; s < 7; ++s) {
            int kb = s + rot; kb = kb >= 7 ? kb - 7 : kb;
            const unsigned d_ = lds0 + (s == 6 ? 0 : s) * A_SLOT;
            if (s == 6) { while (lflag[4] < 4u) __builtin_amdgcn_s_sleep(1); }
            glds16(xb + kb * 128, xo0, d_ + L * 4160); glds16(xb + kb * 128, xo1, d_ + L * 4160 + 1040);
            glds16(xb + kb * 128, xo2, d_ + L * 4160 + 2080); glds16(xb + kb * 128, xo3, d_ + L * 4160 + 3120);
            if ((lane & 7) != 7) { glds16(wkv + (size_t)kb * (64 * 2688), woff, d_ + A_XW + L * A_KP); glds16(wkv + (size_t)kb * (64 * 2688), woff2, d_ + A_XW + L * A_KP + 1088); }
            if (s == 0) BAR();
            else { VMWAIT(6); if (lane == 0) __hip_atomic_fetch_add((LAS unsigned*)&lflag[8 + s - 1], 1u, __ATOMIC_RELAXED, __HIP_MEMORY_SCOPE_WORKGROUP); }
        }
        VMWAIT(0);
        if (lane == 0) __hip_atomic_fetch_add((LAS unsigned*)&lflag[8 + 6], 1u, __ATOMIC_RELAXED, __HIP_MEMORY_SCOPE_WORKGROUP);
        while (lflag[5] < 4u) __builtin_amdgcn_s_sleep(1);
        {
            const char* wq = (const char*)wqkv + (size_t)(16 * L) * 2688 + ((56 * h) & ~15);
            const unsigned qoff = (lane >> 2) * 2688 + (lane & 3) * 16;
#pragma unroll 1
            for (int i = 0; i < 7; ++i) { int kb = i + rot; kb = kb >= 7 ? kb - 7 : kb; glds16(wq + (size_t)kb * (64 * 2688), qoff, lds0 + A_WQ + (4 * i + L) * 1024); }
        }
        const char* psrc; int ppitch, pdst, pstep, k0;
        if (b < 112) { const int cp = b >> 2; k0 = (b & 3) * 7; psrc = (const char*)w1 + cp * 128; ppitch = FF * 2; pdst = W1_OFF + (cp * 2 * 28) * KB; pstep = 28 * KB; }
        else if (b < 224) { const int q = b - 112, cp = q >> 4; k0 = (q & 15) * 7; psrc = (const char*)w2 + cp * 128; ppitch = DM * 2; pdst = W2_OFF + (cp * 2 * 112) * KB; pstep = KB; }
        else { k0 = (b - 224) * 7; psrc = (const char*)wo; ppitch = DM * 2; pdst = WO_OFF; pstep = 4 * KB; }
        int pks[2], pd[2];
#pragma unroll
        for (int i = 0; i < 2; ++i) {
            const int kk = k0 + ((L + 4 * i < 7) ? L + 4 * i : L);
            const int cp = kk >> 5, ks = kk & 31;
            pks[i] = (b >= 224) ? (ks & 1) : 0; pd[i] = (b >= 224) ? pdst + (cp * 64 + (ks >> 2) * 8 + (ks & 3)) * KB : pdst + (b >= 112 ? 2 * kk : kk) * KB;
#pragma unroll
            for (int u = 0; u < 2; ++u) {
                const int row = 8 * u + (lane >> 3), c = 16 * (ks & 1) + row;
                const int srow = (b >= 224) ? (ks >> 1) * DH + (c < DH ? c : DH - 1) : 16 * kk + row;
                glds16(psrc, (unsigned)(srow * ppitch + ((b >= 224) ? cp * 128 : 0) + (lane & 7) * 16), lds0 + A_PT + ((L * 2 + i) * 2 + u) * 1024);
            }
        }
        VMWAIT(4);
        float cq = 0.f;
        LAS const char* tq = lds + A_WQ + L * 1024 + (8 * hi + trq) * 64 + trc + ((56 * h) & 15);
#pragma unroll
        for (int i = 0; i < 7; ++i) cq = sum8(trfrag(tq + i * 4096, 256), cq);
        csl[(2 * 32 + r) * 8 + 2 * L + hi] = cq;
        BAR();
        tag = __builtin_amdgcn_readfirstlane(lflag[2]);
        LAS const char* tp = lds + A_PT + L * 4096 + (4 * hi + trq) * 128 + trc;
        VMWAIT(0);
#pragma unroll
        for (int i = 0; i < 2; ++i) {
            if (L + 4 * i < 7) {
#pragma unroll
                for (int ct = 0; ct < 2; ++ct) {
                    u32x4 f = trfrag(tp + i * 2048 + ct * 64, 1024);
                    if (pks[i] && hi) { f.z = 0u; f.w = 0u; }
                    stf(f, rs, l16, pd[i] + ct * pstep);
                }
            }
        }
    } else {
        const int rt = rb * 4 + w;
        unsigned ftch = flag_ld(ws, ((b * 4 + w) & 255) * 32);
        const unsigned gen = flag_ld(ws, FW_GEN);
        if (tid < 32) lflag[tid] = 0u;
        if ((lane & 7) == 7) {
#pragma unroll
            for (int s = 0; s < 12; ++s) *(LAS u32x4*)(lds + (s >> 1) * A_SLOT + A_XW + w * A_KP + (s & 1) * 1088 + l16) = u32x4{ONES, ONES, ONES, ONES};
        }
        BAR();
        LAS const char* xr = lds + (8 * (w >> 1) + (r & 7)) * 1040 + (4 * (w & 1) + (r >> 3)) * 128 + hi * 16;
        LAS const char* tw = lds + A_XW + (4 * hi + (trq >> 1)) * 128 + (trq & 1) * 1088 + trc;
        u32x4 xf[28];
        f32x16 a0 = {}, a1 = {};
        float s2 = 0.f, c0 = 0.f, c1 = 0.f;
        u32x4 wa[2][4], wb[2][4];
#define A_WAIT(s) { while (lflag[8 + (s)] < 4u) {} asm volatile("" ::: "memory"); }
#define A_RDX(s, i) { xf[4 * (s) + i] = *(LAS const u32x4*)(xr + ((s) % 6) * A_SLOT + (i ^ w) * 32); wa[(s) & 1][i] = trfrag(tw + ((s) % 6) * A_SLOT + (i ^ w) * A_KP, 256); SB(); }
#define A_RDW(s, i) { wb[(s) & 1][i] = trfrag(tw + ((s) % 6) * A_SLOT + (i ^ w) * A_KP + 56, 256); SB(); }
        unsigned nf = 0u;
#define A_STAGE(s, DONE) { if ((s) < 6) { if (nf < 4u) A_WAIT((s) + 1) asm volatile("" ::: "memory"); } \
        if ((DONE) && lane == 0) __hip_atomic_fetch_add((LAS unsigned*)&lflag[DONE], 1u, __ATOMIC_RELAXED, __HIP_MEMORY_SCOPE_WORKGROUP); \
        _Pragma("unroll") for (int i = 0; i < 4; ++i) { \
            a0 = MFMA(wa[(s) & 1][i], xf[4 * (s) + i], a0); PIN(a0); SB(); if ((s) < 6) A_RDX(((s) < 6 ? (s) + 1 : 6), i) \
            a1 = MFMA(wb[(s) & 1][i], xf[4 * (s) + i], a1); PIN(a1); SB(); if ((s) < 6) A_RDW(((s) < 6 ? (s) + 1 : 6), i) } \
        if ((s) < 5) nf = lflag[8 + ((s) < 5 ? (s) + 2 : 6)]; \
        SB(); \
        _Pragma("unroll") for (int i = 0; i < 4; ++i) s2 = sq8(xf[4 * (s) + i], s2); \
        c0 = sum8(wa[(s) & 1][0], c0); c1 = sum8(wb[(s) & 1][0], c1); PIN(s2); PIN(c0); PIN(c1); SB(); }
        A_WAIT(0)
#pragma unroll
        for (int i = 0; i < 4; ++i) { A_RDX(0, i) A_RDW(0, i) }
        A_STAGE(0, 4)
        A_STAGE(1, 0) A_STAGE(2, 5) A_STAGE(3, 0)
        A_STAGE(4, 0) A_STAGE(5, 0)
        csl[(0 * 32 + r) * 8 + 2 * w + hi] = c0; csl[(1 * 32 + r) * 8 + 2 * w + hi] = c1;
        asm volatile("s_waitcnt lgkmcnt(0)" ::: "memory");
        if (lane == 0) __hip_atomic_fetch_add((LAS unsigned*)&lflag[6], 1u, __ATOMIC_RELAXED, __HIP_MEMORY_SCOPE_WORKGROUP);
        c0 = 0.f; c1 = 0.f;
#pragma unroll
        for (int i = 0; i < 4; ++i) { s2 = sq8(xf[24 + i], s2); c0 = sum8(wa[0][i], c0); c1 = sum8(wb[0][i], c1); }
        PIN(s2); PIN(c0); PIN(c1); SB();
#pragma unroll
        for (int i = 0; i < 4; ++i) a1 = MFMA(wb[0][i], xf[24 + i], a1);
        PIN(a1); SB();
        while (lflag[6] < 4u) {}
        asm volatile("" ::: "memory");
#define A_CS(t) ({ const f32x4 p0 = *(LAS const f32x4*)(csl + ((t) * 32 + r) * 8), p1 = *(LAS const f32x4*)(csl + ((t) * 32 + r) * 8 + 4); ((p0.x + p0.y) + (p0.z + p0.w)) + ((p1.x + p1.y) + (p1.z + p1.w)); })
#pragma unroll
        for (int i = 0; i < 4; ++i) a0 = MFMA(wa[0][i], xf[24 + i], a0);
        tag = __builtin_amdgcn_readfirstlane(gen) * 1664525u + 1013904223u;
        PIN(ftch);
        if (tid == 0) lflag[2] = tag;
        s2 = swap_add(s2);
        const float mu = swap_add(hi ? a1[12] : 0.f) * (1.f / DM), var = fmaxf(s2 * (1.f / DM) - mu * mu, 0.f), rstd = __builtin_amdgcn_rsqf(var + EPS);
        const u32x4 cb = corr_b(mu);
        const int dst = KV_OFF + (((2 * h) & 15) * 64 + rt) * 4 * KB + (h < 8 ? 0 : 2 * KB);
        const float cs0 = A_CS(0) + swap_add(c0), cs1 = A_CS(1) + swap_add(c1);
        a1 = MFMA(corr_a(cs1, hi), cb, a1); a0 = MFMA(corr_a(cs0, hi), cb, a0);
        a0 *= rstd; a1 *= rstd;
        u32x4 f00 = pack8(a0, 0), f01 = pack8(a0, 1), f10 = pack8(a1, 0), f11 = pack8(a1, 1);
        const unsigned pz = h < 8 ? ONES : 0x00003f80u, pw = h < 8 ? 0x00003f80u : 0u;
        if (hi) { f01.z = pz; f01.w = pw; f11.z = pz; f11.w = pw; }
        if (h >= 8) {
            LAS char* tb = lds + 4 * A_SLOT + w * 4096;
            LAS char* wr = tb + r * 64 + hi * 8;
            *(LAS u32x2*)(wr) = u32x2{f00.x, f00.y}; *(LAS u32x2*)(wr + 16) = u32x2{f00.z, f00.w}; *(LAS u32x2*)(wr + 32) = u32x2{f01.x, f01.y}; *(LAS u32x2*)(wr + 48) = u32x2{f01.z, f01.w};
            *(LAS u32x2*)(wr + 2048) = u32x2{f10.x, f10.y}; *(LAS u32x2*)(wr + 2064) = u32x2{f10.z, f10.w}; *(LAS u32x2*)(wr + 2080) = u32x2{f11.x, f11.y}; *(LAS u32x2*)(wr + 2096) = u32x2{f11.z, f11.w};
            asm volatile("s_waitcnt lgkmcnt(0)" ::: "memory");
            LAS const char* rd = tb + (4 * hi + trq) * 64 + trc;
            f00 = trfrag(rd, 512); f01 = trfrag(rd + 1024, 512); f10 = trfrag(rd + 2048, 512); f11 = trfrag(rd + 3072, 512);
        }
        stf(f00, rs, l16, dst); stf(f01, rs, l16 + KB, dst); stf(f10, rs, l16, dst + 64 * 4 * KB); stf(f11, rs, l16 + KB, dst + 64 * 4 * KB);
        VMWAIT(0);
        if (lane == 0 && __hip_atomic_fetch_add((LAS unsigned*)&lflag[7], 1u, __ATOMIC_RELAXED, __HIP_MEMORY_SCOPE_WORKGROUP) == 3u) flag_st(ws, FW_KV + h * 32 + rb, tag);
        BAR();
        LAS const char* tq = lds + A_WQ + (8 * hi + trq) * 64 + trc + ((56 * h) & 15);
        f32x16 aq = {};
        u32x4 wq[8];
#pragma unroll
        for (int i = 0; i < 8; ++i) wq[i] = trfrag(tq + (i >> 2) * 4096 + ((i & 3) ^ w) * 1024, 256);
        SB();
#pragma unroll
        for (int i = 0; i < 28; ++i) {
            aq = MFMA(wq[i & 7], xf[i], aq); SB();
            if (i + 8 < 28) wq[i & 7] = trfrag(tq + ((i + 8) >> 2) * 4096 + ((i & 3) ^ w) * 1024, 256);
            SB();
        }
        aq = MFMA(corr_a(A_CS(2), hi), cb, aq);
        aq *= rstd * QSCALE;
        q0 = pack8(aq, 0); q1 = pack8(aq, 1);
        if (hi) { q1.z = 0u; q1.w = 0u; }
    }
    const bool post = bi < 28;
    const int gam = 4 * xcd + bi / 7, c7 = bi % 7;
    const int rtA = (gam >> 1) * 4 + (gam & 1);
    const __amdgpu_buffer_rsrc_t rw = __builtin_amdgcn_make_buffer_rsrc((void*)ws, 0, post ? WS_BYTES : 0, 0x00020000);
    u32x4 R[32];
    const int kvo = KV_OFF + (h * 64 + w * 8) * 4 * KB;
#define KVL(i) ldf(rs, l16 + ((i) & 3) * KB, kvo + (((((i) >> 2) + rb) & 7) << 12))
    {
        const int fw = FW_KV + ((h >> 1) + 8 * (lane & 1)) * 32 + 2 * w + ((lane >> 1) & 1);
        poll_flags(ws, fw, tag, tag + 1u);
#pragma unroll
        for (int i = 0; i < 8; ++i) { R[i] = KVL(i); SB(); }
        if (w < 4) {
            f32x16 sd = {};
            sd = MFMA(R[0], q0, sd); sd = MFMA(R[1], q1, sd);
            const float m = swap_max(max16(sd));
            if (hi) { const u32x2 p = split3(m); q1.z = p.x; q1.w = p.y; }
            *(LAS u32x4*)(lds + B_QS + w * 2048 + l16) = q0; *(LAS u32x4*)(lds + B_QS + w * 2048 + 1024 + l16) = q1;
        }
    }
    BAR();
    if (tid == 256) flag_st(ws, FW_W + b, tag);
    int woo = WO_OFF + (c7 * 8 + w) * 8 * KB, w1o = W1_OFF + (8 * c7 + w) * 28 * KB, w2o = W2_OFF + (c7 * 8 + w) * 28 * KB;
    int ofo = OF_OFF + (rtA * 32 + 4 * w) * KB, ufo = UF_OFF + (rtA * 112 + 14 * w) * KB;
    SPIN(woo); SPIN(w1o); SPIN(w2o); SPIN(ofo); SPIN(ufo);
#define EXP4(S, i) { S[i] = __builtin_amdgcn_exp2f(S[i]); S[i + 1] = __builtin_amdgcn_exp2f(S[i + 1]); S[i + 2] = __builtin_amdgcn_exp2f(S[i + 2]); S[i + 3] = __builtin_amdgcn_exp2f(S[i + 3]); }
#define HOOK(PASS, i) if ((i) >= 32) { glds16c(ws + w1o + (16 + (i) - 32) * KB, l16, lds0 + B_W1 + (w * 7 + (i) - 32) * KB); } \
        else if ((i) >= 0) R[(i) & 31] = (PASS) ? (((i) & 31) < 8 ? LDK(rw, woo, (i) & 31) : LDK(rw, w1o, ((i) & 31) - 8)) : KVL((i) & 31);
#define STEP(PASS, C, N, OC, OP, QN0, QN1, t, tn, tp, FIRST, LAST, L0, L1, L2, L3) { \
        if (!(LAST)) { N = MFMA(R[4 * (tn)], QN0, zero16); SB(); } \
        HOOK(PASS, L0) \
        EXP4(C, 0) p0.x = pk2(C[0], C[1]); p0.y = pk2(C[2], C[3]); PIN(p0); SB(); \
        if (!(FIRST)) { OP = MFMA(R[4 * (tp) + 3], p1, OP); SB(); } \
        HOOK(PASS, L1) \
        EXP4(C, 4) p0.z = pk2(C[4], C[5]); p0.w = pk2(C[6], C[7]); PIN(p0); SB(); \
        if (!(LAST)) { N = MFMA(R[4 * (tn) + 1], QN1, N); SB(); } \
        HOOK(PASS, L2) \
        EXP4(C, 8) p1.x = pk2(C[8], C[9]); p1.y = pk2(C[10], C[11]); PIN(p1); SB(); \
        OC = MFMA(R[4 * (t) + 2], p0, OC); SB(); \
        HOOK(PASS, L3) \
        EXP4(C, 12) p1.z = pk2(C[12], C[13]); p1.w = pk2(C[14], C[15]); PIN(p1); SB(); }
#define TILE(PASS, t, A0, A1, A2, A3, B0, B1, B2, B3) \
        STEP(PASS, sa, sb, oa, ob, qb0, qb1, t, t, (t) - 1 < 0 ? 0 : (t) - 1, (t) == 0, false, A0, A1, A2, A3) \
        STEP(PASS, sb, sa, ob, oa, qa0, qa1, t, (t) + 1 > 7 ? 7 : (t) + 1, t, false, (t) == 7, B0, B1, B2, B3)
#define TILE0(t) TILE(0, t, 4 * (t) + 8, -1, 4 * (t) + 9, -1, 4 * (t) + 10, -1, 4 * (t) + 11, -1)
#define COMBINE(PASS) { \
        LAS const float* lp = (LAS const float*)(lds + B_RED) + (((w >> 2) * 4 + 3) * 64 + 32 + r) * 4; \
        float lv[8]; _Pragma("unroll") for (int i = 0; i < 8; ++i) lv[i] = lp[i * 8 * 64 * 4]; \
        const f32x4 o = red_sum(lds, w, lane); \
        const float il = __builtin_amdgcn_rcpf(((lv[0] + lv[1]) + (lv[2] + lv[3])) + ((lv[4] + lv[5]) + (lv[6] + lv[7]))); \
        const int qt = rb * 4 + (PASS) * 2 + (w >> 2); \
        sth(u32x2{pk2(o[0] * il, o[1] * il), pk2(o[2] * il, o[3] * il)}, rs, lane, w, OF_OFF + ((qt * 16 + h) * 2 + ((w >> 1) & 1)) * KB); }
#define LADD(i) __hip_atomic_fetch_add((LAS unsigned*)&lflag[i], 1u, __ATOMIC_RELAXED, __HIP_MEMORY_SCOPE_WORKGROUP)
#define COMBINEL(u) { \
        LAS const float* lp = (LAS const float*)(lds + B_RED) + ((((u) >> 2) * 4 + 3) * 64 + 32 + r) * 4; \
        LAS const f32x4* cp = (LAS const f32x4*)(lds + B_RED) + (((u) & 6) + hi) * 64 + 32 * ((u) & 1) + r; \
        f32x4 o = (cp[0] + cp[512]) + (cp[1024] + cp[1536]); float l = (lp[0] + lp[2048]) + (lp[4096] + lp[6144]); PIN(o); PIN(l); SB(); \
        o += (cp[2048] + cp[2560]) + (cp[3072] + cp[3584]); l += (lp[8192] + lp[10240]) + (lp[12288] + lp[14336]); \
        const float il = __builtin_amdgcn_rcpf(l); \
        sth(u32x2{pk2(o[0] * il, o[1] * il), pk2(o[2] * il, o[3] * il)}, rs, lane, u, OF_OFF + (((rb * 4 + ((u) >> 2)) * 16 + h) * 2 + (((u) >> 1) & 1)) * KB); SB(); }
#define FLAG_O(PASS) { if (tid < 2) flag_st(ws, FW_RT + (0 * 64 + rb * 4 + (PASS) * 2 + tid) * 32 + h, tag); }
#define LOADQ(PASS) \
        qa0 = *(LAS const u32x4*)(lds + B_QS + (PASS) * 4096 + l16); qa1 = *(LAS const u32x4*)(lds + B_QS + (PASS) * 4096 + 1024 + l16); \
        qb0 = *(LAS const u32x4*)(lds + B_QS + (PASS) * 4096 + 2048 + l16); qb1 = *(LAS const u32x4*)(lds + B_QS + (PASS) * 4096 + 3072 + l16);
#define PASS_BEGIN(PASS) LOADQ(PASS) oa = zero16; ob = zero16; sa = MFMA(R[0], qa0, zero16); sa = MFMA(R[1], qa1, sa); SB();
#define PASS_END(PASS) \
        ob = MFMA(R[31], p1, ob); \
        { const bool bad = hi && !(oa[12] < 1.0e18f && ob[12] < 1.0e18f); \
          if (__any(bad) && lane == 0) lflag[PASS] = 1u; }
    u32x2 xres0, xres1;
    unsigned pfo;
    {
        const f32x16 zero16 = {};
        u32x4 qa0, qa1, qb0, qb1, p0, p1; f32x16 oa, ob, sa, sb;
        __builtin_amdgcn_s_setprio(3);
        PASS_BEGIN(0)
        TILE0(0) TILE0(1) TILE0(2) TILE0(3)
        __builtin_amdgcn_s_setprio(2);
        TILE0(4) TILE0(5)
        TILE(0, 6, -1, -1, -1, -1, -1, -1, -1, -1)
        const u32x4 wfl = __builtin_amdgcn_raw_buffer_load_b128(rs, l16, FW_W * 4, SC1);
        TILE(0, 7, -1, -1, -1, -1, -1, -1, -1, -1)
        __builtin_amdgcn_s_setprio(3);
        PASS_END(0)
        red_write(lds, w, lane, oa, ob);
        asm volatile("s_waitcnt lgkmcnt(0)" ::: "memory");
        if (lane == 0) LADD(16);
        {
            u32x4 v = wfl;
            for (unsigned spins = 0; !__all(v.x == tag && v.y == tag && v.z == tag && v.w == tag) && spins < (1u << 17); ++spins) {
                asm volatile("" ::: "memory");
                v = __builtin_amdgcn_raw_buffer_load_b128(rs, l16, FW_W * 4, SC1);
            }
            __builtin_amdgcn_fence(__ATOMIC_ACQUIRE, "wavefront");
        }
        PASS_BEGIN(1)
        __builtin_amdgcn_s_setprio(1);
        TILE(1, 0, 0, 32, 1, 33, 34, 35, 36, 2)
        TILE(1, 1, 4, 3, 5, 37, 38, -1, -1, 6)
        TILE(1, 2, 8, 7, 9, -1, -1, -1, -1, -1)
        TILE(1, 3, -1, -1, -1, -1, -1, -1, -1, -1)
        __builtin_amdgcn_s_setprio(0);
        if (w < 4) {
            lds_wait(lds0 + B_FLAG + 64, 8u);
            __builtin_amdgcn_s_setprio(3);
            const bool okA = lflag[0] == 0u;
            if (okA) { COMBINEL(w) COMBINEL(w + 4) }
            asm volatile("s_waitcnt lgkmcnt(0)" ::: "memory");
            if (lane == 0) LADD(17);
            VMWAIT(0);
            if (lane == 0 && LADD(18) == 3u && okA) { flag_st(ws, FW_RT + (rb * 4) * 32 + h, tag); flag_st(ws, FW_RT + (rb * 4 + 1) * 32 + h, tag); }
            __builtin_amdgcn_s_setprio(0);
        }
        TILE(1, 4, 10, 11, 12, 13, 14, 15, 16, 17)
        TILE(1, 5, 18, -1, 19, -1, 20, 21, -1, 22)
        {
            const unsigned short* xp = x + (size_t)((post ? rtA : 0) * 32 + r) * DM + c7 * 64 + (w >> 1) * 16 + 8 * hi + 4 * (w & 1);
            xres0 = *(const u32x2*)xp; xres1 = *(const u32x2*)(xp + 64 * DM);
        }
        TILE(1, 6, -1, 23, -1, -1, -1, -1, -1, -1)
        pfo = flag_ld(ws, FW_RT + (0 * 64 + (post ? rtA : 0)) * 32 + 2 * w + (lane & 1));
        TILE(1, 7, -1, -1, -1, -1, -1, -1, -1, -1)
        PASS_END(1)
        lds_wait(lds0 + B_FLAG + 68, 4u);
        red_write(lds, w, lane, oa, ob);
        BAR();
        const bool fb = lflag[0] != 0u || lflag[1] != 0u;
        if (__builtin_expect(fb, 0)) {
            if (lflag[1] == 0u) COMBINE(1)
#pragma unroll 1
            for (int p = 0; p < 2; ++p) {
                if (lflag[p] == 0u) continue;
                LOADQ(p)
                float ma = -INFINITY, mb = -INFINITY;
#pragma unroll 1
                for (int t = 0; t < 8; ++t) {
                    const u32x4 k0 = ldf(rs, l16, kvo + t * 4 * KB), k1 = ldf(rs, l16, kvo + t * 4 * KB + KB);
                    sa = MFMA(k0, qa0, zero16); sa = MFMA(k1, qa1, sa); sb = MFMA(k0, qb0, zero16); sb = MFMA(k1, qb1, sb);
                    ma = fmaxf(ma, max16(sa)); mb = fmaxf(mb, max16(sb));
                }
                ma = swap_max(ma); mb = swap_max(mb);
                LAS float* mx = (LAS float*)(lds + B_ST);
                mx[w * 64 + lane] = hi ? mb : ma;
                BAR();
#pragma unroll
                for (int i = 0; i < 8; ++i) { ma = fmaxf(ma, mx[i * 64 + r]); mb = fmaxf(mb, mx[i * 64 + 32 + r]); }
                if (hi) {
                    const u32x2 pa = split3(ma - (bflo(qa1.z) + bfhi(qa1.z) + bflo(qa1.w))), pb = split3(mb - (bflo(qb1.z) + bfhi(qb1.z) + bflo(qb1.w)));
                    qa1.z = pa.x; qa1.w = pa.y; qb1.z = pb.x; qb1.w = pb.y;
                }
                oa = zero16; ob = zero16;
#pragma unroll 1
                for (int t = 0; t < 8; ++t) {
                    const u32x4 k0 = ldf(rs, l16, kvo + t * 4 * KB), k1 = ldf(rs, l16, kvo + t * 4 * KB + KB), v0 = ldf(rs, l16, kvo + t * 4 * KB + 2 * KB), v1 = ldf(rs, l16, kvo + t * 4 * KB + 3 * KB);
                    sa = MFMA(k0, qa0, zero16); sa = MFMA(k1, qa1, sa); sb = MFMA(k0, qb0, zero16); sb = MFMA(k1, qb1, sb);
                    EXP4(sa, 0) EXP4(sa, 4) EXP4(sa, 8) EXP4(sa, 12) EXP4(sb, 0) EXP4(sb, 4) EXP4(sb, 8) EXP4(sb, 12)
                    oa = MFMA(v0, pack8(sa, 0), oa); oa = MFMA(v1, pack8(sa, 1), oa); ob = MFMA(v0, pack8(sb, 0), ob); ob = MFMA(v1, pack8(sb, 1), ob);
                }
                red_write(lds, w, lane, oa, ob);
                BAR();
                COMBINE(p)
                VMBAR(0);
                if (p == 0) FLAG_O(0)
            }
        }
        if (post) {
            poll_flags(ws, FW_RT + (0 * 64 + rtA) * 32 + 2 * w + (lane & 1), tag, pfo);
#pragma unroll
            for (int i = 0; i < 4; ++i) R[24 + i] = LDK(rs, ofo, i);
        }
        if (!fb) COMBINE(1)
    }
    if (!post) { VMBAR(0); FLAG_O(1) return; }
    const int rtB = rtA + 2;
    u32x4 E[5], of[4];
    f32x16 a0, a1;
#define WO_LOAD(rt) { _Pragma("unroll") for (int i = 0; i < 4; ++i) of[i] = LDK(rs, ofo + 64 * KB, i); SB(); }
#define WO_MFMA() { a0 = f32x16{}; a1 = f32x16{}; _Pragma("unroll") for (int i = 0; i < 4; ++i) { a0 = MFMA(R[i], of[i], a0); a1 = MFMA(R[4 + i], of[i], a1); } }
#define WO_FIN(rt, xr, hr) { const f32x4 v = red_sum(lds, w, lane); \
        hr = u32x2{pk2(v[0] + bflo(xr.x), v[1] + bfhi(xr.x)), pk2(v[2] + bflo(xr.y), v[3] + bfhi(xr.y))}; \
        sth(hr, rs, lane, w, HF_OFF + ((rt) * 28 + c7 * 4 + (w >> 1)) * KB); }
#define W2R(i) ((i) < 8 ? 24 + (i) : (i) - 8)
#define W2_LOAD(lo, hi_) { _Pragma("unroll") for (int i = (lo); i < (hi_); ++i) R[W2R(i)] = LDK(rs, w2o, i); SB(); }
    u32x2 hres0, hres1;
#pragma unroll
    for (int i = 0; i < 4; ++i) of[i] = R[24 + i];
    WO_MFMA()
    VMBAR(0);
    FLAG_O(1)
#pragma unroll
    for (int i = 0; i < 5; ++i) E[i] = LDK(rs, w1o, 23 + i);
    W2_LOAD(0, 4)
    red_write(lds, w, lane, a0, a1);
    BAR();
    WO_FIN(rtA, xres0, hres0)
    float cs1 = 0.f;
    LAS const char* w1l = lds + B_W1 + w * 7 * KB + l16;
#pragma unroll
    for (int i = 0; i < 16; ++i) cs1 = sum8(R[8 + i], cs1);
#pragma unroll
    for (int i = 0; i < 7; ++i) cs1 = sum8(*(LAS const u32x4*)(w1l + i * KB), cs1);
    PIN(cs1); SB();
    poll_flags(ws, FW_RT + (0 * 64 + rtB) * 32 + 2 * w + (lane & 1), tag, tag + 1u);
    WO_LOAD(rtB)
    BAR();
    if (tid == 0) flag_st(ws, FW_RT + (1 * 64 + rtA) * 32 + c7, tag);
    W2_LOAD(4, 8)
    WO_MFMA()
    red_write(lds, w, lane, a0, a1);
    BAR();
    WO_FIN(rtB, xres1, hres1)
#pragma unroll
    for (int i = 0; i < 5; ++i) cs1 = sum8(E[i], cs1);
    const u32x4 ca = corr_a(swap_add(cs1), hi);
    const int wp = w < 7 ? w : 6;
#define H_LOAD(rt) { _Pragma("unroll") for (int i = 0; i < 4; ++i) of[i] = LDK(rs, HF_OFF + ((rt) * 28 + 4 * wp) * KB, i); SB(); }
#define H_STAGE(hs, ab) { float s1 = 0.f, s2 = 0.f; \
        _Pragma("unroll") for (int i = 0; i < 4; ++i) { *(LAS u32x4*)(lds + (hs) + l16 + (4 * wp + i) * KB) = of[i]; s1 = sum8(of[i], s1); s2 = sq8(of[i], s2); } \
        s1 = swap_add(s1); s2 = swap_add(s2); ((LAS f32x2*)(lds + B_ST))[(ab) * 224 + wp * 32 + r] = f32x2{s1, s2}; }
    float rstd2;
#define F1_MFMA(hs, ab, HOOK) { u32x4 hb[8], wl[4], cb2; f32x2 sv[7]; a0 = f32x16{}; \
        _Pragma("unroll") for (int i = 0; i < 8; ++i) hb[i] = *(LAS const u32x4*)(lds + (hs) + l16 + i * KB); \
        SB(); \
        _Pragma("unroll") for (int i = 0; i < 28; ++i) { \
            a0 = MFMA(i < 16 ? R[8 + (i < 16 ? i : 0)] : i < 23 ? wl[i & 3] : E[i < 23 ? 0 : i - 23], hb[i & 7], a0); SB(); \
            if (i + 8 < 28) hb[i & 7] = *(LAS const u32x4*)(lds + (hs) + l16 + (i + 8) * KB); \
            if (i + 4 >= 16 && i + 4 < 23) wl[i & 3] = *(LAS const u32x4*)(w1l + (i + 4 - 16) * KB); \
            if (i == 20) { _Pragma("unroll") for (int k = 0; k < 7; ++k) sv[k] = ((LAS const f32x2*)(lds + B_ST))[(ab) * 224 + k * 32 + r]; } \
            if (i == 24) { const f32x2 t = ((sv[0] + sv[1]) + (sv[2] + sv[3])) + ((sv[4] + sv[5]) + sv[6]); const float mu = t.x * (1.f / DM); \
                rstd2 = __builtin_amdgcn_rsqf(fmaxf(t.y * (1.f / DM) - mu * mu, 0.f) + EPS); cb2 = corr_b(mu); PIN(cb2); PIN(rstd2); } \
            HOOK; SB(); } \
        a0 = MFMA(ca, cb2, a0); SB(); }
#define F1_FIN(rt) { const float nr = -LOG2E * rstd2; const int ud = UF_OFF + ((rt) * 112 + (8 * c7 + w) * 2) * KB; \
        _Pragma("unroll") for (int i = 0; i < 8; ++i) a0[i] = a0[i] * rstd2 * __builtin_amdgcn_rcpf(1.f + __builtin_amdgcn_exp2f(a0[i] * nr)); \
        stf(pack8(a0, 0), rs, l16, ud); \
        _Pragma("unroll") for (int i = 8; i < 16; ++i) a0[i] = a0[i] * rstd2 * __builtin_amdgcn_rcpf(1.f + __builtin_amdgcn_exp2f(a0[i] * nr)); \
        stf(pack8(a0, 1), rs, l16 + KB, ud); }
    poll_flags(ws, FW_RT + (1 * 64 + rtA) * 32 + wp, tag, tag + 1u);
    H_LOAD(rtA)
    H_STAGE(B_HS, 0)
    BAR();
    if (tid == 0) flag_st(ws, FW_RT + (1 * 64 + rtB) * 32 + c7, tag);
    unsigned pf;
    F1_MFMA(B_HS, 0, if (i < 8) R[i] = LDK(rs, w2o, i + 8); if (i == 20) pf = flag_ld(ws, FW_RT + (1 * 64 + rtB) * 32 + wp))
    poll_flags(ws, FW_RT + (1 * 64 + rtB) * 32 + wp, tag, pf);
    H_LOAD(rtB)
    F1_FIN(rtA)
    H_STAGE(B_RED, 1)
    VMBAR(0);
    if (tid == 0) flag_st(ws, FW_RT + (2 * 64 + rtA) * 32 + c7, tag);
    F1_MFMA(B_RED, 1, if (i < 12) R[8 + (i < 12 ? i : 0)] = LDK(rs, w2o, i + 16))
    const int fu = (7 * w + 6 * (lane & 1)) >> 3;
    pf = flag_ld(ws, FW_RT + (2 * 64 + rtA) * 32 + fu);
    F1_FIN(rtB)
    VMBAR(0);
    if (tid == 0) flag_st(ws, FW_RT + (2 * 64 + rtB) * 32 + c7, tag);
    u32x4 uf[8];
    f32x16 b0, b1;
#define U_LD(j) uf[(j) & 7] = LDK(rs, ufo + ((j) / 14) * 224 * KB, (j) % 14)
#define F2_FIN(red_, hr, o_) { const f32x4 v = red_sum(lds + (red_), w, lane); \
        o_ = u32x2{pk2(v[0] + bflo(hr.x), v[1] + bfhi(hr.x)), pk2(v[2] + bflo(hr.y), v[3] + bfhi(hr.y))}; }
    poll_flags(ws, FW_RT + (2 * 64 + rtA) * 32 + fu, tag, pf);
#pragma unroll
    for (int j = 0; j < 8; ++j) U_LD(j);
    SB();
    a0 = f32x16{}; a1 = f32x16{};
#pragma unroll
    for (int j = 0; j < 6; ++j) {
        a0 = MFMA(R[W2R(2 * j)], uf[j & 7], a0); a1 = MFMA(R[W2R(2 * j + 1)], uf[j & 7], a1); SB(); U_LD(j + 8);
        if (j == 3) pf = flag_ld(ws, FW_RT + (2 * 64 + rtB) * 32 + fu);
        SB();
    }
    poll_flags(ws, FW_RT + (2 * 64 + rtB) * 32 + fu, tag, pf);
#pragma unroll
    for (int j = 6; j < 14; ++j) { a0 = MFMA(R[W2R(2 * j)], uf[j & 7], a0); a1 = MFMA(R[W2R(2 * j + 1)], uf[j & 7], a1); SB(); U_LD(j + 8); SB(); }
    red_write(lds, w, lane, a0, a1);
    SB();
    b0 = f32x16{}; b1 = f32x16{};
#pragma unroll
    for (int j = 14; j < 28; ++j) { b0 = MFMA(R[W2R(2 * (j - 14))], uf[j & 7], b0); b1 = MFMA(R[W2R(2 * (j - 14) + 1)], uf[j & 7], b1); SB(); if (j + 8 < 28) U_LD(j + 8 < 28 ? j + 8 : 27); SB(); }
    red_write(lds + 64 * KB, w, lane, b0, b1);
    BAR();
    u32x2 oA, oB;
    F2_FIN(0, hres0, oA)
    F2_FIN(64 * KB, hres1, oB)
    unsigned short* op = out + (size_t)(rtA * 32 + r) * DM + c7 * 64 + (w >> 1) * 16 + 8 * hi + 4 * (w & 1);
    *(u32x2*)op = oA; *(u32x2*)(op + 64 * DM) = oB;
    if (b == 0 && tid == 0) flag_st(ws, FW_GEN, tag);

}

extern "C" void kernel_launch(void* const* d_in, const int* in_sizes, int n_in, void* d_out, int out_size, void* d_ws, size_t ws_size, hipStream_t stream) {
    (void)hipFuncSetAttribute((const void*)fused_block, hipFuncAttributeMaxDynamicSharedMemorySize, LDS_BYTES);
    fused_block<<<256, 512, LDS_BYTES, stream>>>((const unsigned short*)d_in[0], (const unsigned short*)d_in[1], (const unsigned short*)d_in[2], (const unsigned short*)d_in[4],
                                                 (const unsigned short*)d_in[5], (char*)d_ws, (unsigned short*)d_out);
}
```
